```python
import torch

def _softmax(x, axis=-1):
    return torch.softmax(x, dim=axis)
def _gelu(x):
    return torch.nn.functional.gelu(x, approximate="tanh")
def _silu(x):
    return torch.nn.functional.silu(x)
def _sigmoid(x):
    return torch.sigmoid(x)
def _layernorm(x, axis=-1, eps=1e-5):
    m = x.mean(dim=axis, keepdim=True)
    v = x.var(dim=axis, keepdim=True, unbiased=False)
    return (x - m) / torch.sqrt(v + eps)
def _scatter_add(tgt, idx, src, axis=0):
    return tgt.clone().index_add_(axis, idx, src)
def _segment_sum(src, seg, n):
    out = torch.zeros((n,) + src.shape[1:], dtype=src.dtype)
    return out.index_add_(0, seg, src)
def _pad(x, pw):
    flat = tuple(v for pair in reversed(pw) for v in pair)
    return torch.nn.functional.pad(x, flat)

kernel_name = 'concat_heads__ssm_conv_s5__linear_kernel_hedgehog__prompt'

def setup_inputs():

    _w = (lambda a: (lambda t: t.to(torch.bfloat16) if t.is_floating_point() else t)(torch.as_tensor(a)))
    def _A_setup():
        B, S, D = 1, 2048, 512
        P = D
        Lambda_re = -0.5 * (lambda _x: torch.exp(torch.as_tensor(_x)))((lambda *s: torch.randn(*s, dtype=torch.bfloat16) * 0.8)(P))
        Lambda_im = (lambda *s: torch.randn(*s, dtype=torch.bfloat16) * 0.8)(P) * 1.0
        B_re = (lambda *s: torch.randn(*s, dtype=torch.bfloat16) * 0.8)(P, D) * (D ** -0.5)
        B_im = (lambda *s: torch.randn(*s, dtype=torch.bfloat16) * 0.8)(P, D) * (D ** -0.5)
        C_re = (lambda *s: torch.randn(*s, dtype=torch.bfloat16) * 0.8)(D, P) * (P ** -0.5)
        C_im = (lambda *s: torch.randn(*s, dtype=torch.bfloat16) * 0.8)(D, P) * (P ** -0.5)
        log_dt = (lambda *s: torch.randn(*s, dtype=torch.bfloat16) * 0.8)(P) * 0.3 - 2.0
        Dskip = (lambda *s: torch.randn(*s, dtype=torch.bfloat16) * 0.8)(D)
        return {
            "x": _w((lambda *s: torch.randn(*s, dtype=torch.bfloat16) * 0.05)(B, S, D)),
            "Lambda_re": _w(Lambda_re),
            "Lambda_im": _w(Lambda_im),
            "B_re": _w(B_re),
            "B_im": _w(B_im),
            "C_re": _w(C_re),
            "C_im": _w(C_im),
            "log_dt": _w(log_dt),
            "Dskip": _w(Dskip),
        }
    _A_in = _A_setup()

    def _B_setup():
        B, S, D, H = 1, 2048, 512, 32
        Dh = D // H
        return {
            "x": _w((lambda *s: torch.randn(*s, dtype=torch.bfloat16) * 0.4)(B, S, D)),
            "wqkv": _w((lambda *s: torch.randn(*s, dtype=torch.bfloat16) * 0.4)(D, 3 * D)),
            "w_phi": _w((lambda *s: torch.randn(*s, dtype=torch.bfloat16) * 0.4)(H, Dh, Dh) * (Dh ** -0.5)),
            "wo": _w((lambda *s: torch.randn(*s, dtype=torch.bfloat16) * 0.05)(D, D)),
        }
    _B_in = _B_setup()

    return {
        "A_x": _A_in["x"],
        "A_Lambda_re": _A_in["Lambda_re"],
        "A_Lambda_im": _A_in["Lambda_im"],
        "A_B_re": _A_in["B_re"],
        "A_B_im": _A_in["B_im"],
        "A_C_re": _A_in["C_re"],
        "A_C_im": _A_in["C_im"],
        "A_log_dt": _A_in["log_dt"],
        "A_Dskip": _A_in["Dskip"],
        "B_x": _B_in["x"],
        "B_wqkv": _B_in["wqkv"],
        "B_w_phi": _B_in["w_phi"],
        "B_wo": _B_in["wo"],
    }

def reference(A_x, A_Lambda_re, A_Lambda_im, A_B_re, A_B_im, A_C_re, A_C_im, A_log_dt, A_Dskip, B_x, B_wqkv, B_w_phi, B_wo):
    def _A_ref(x, Lambda_re, Lambda_im, B_re, B_im, C_re, C_im, log_dt, Dskip):
        Bsz, L, H = x.shape
        Lam = Lambda_re + 1j * Lambda_im
        Bmat = B_re + 1j * B_im
        Cmat = C_re + 1j * C_im
        dt = torch.exp(log_dt)
        Lam_bar = torch.exp(dt * Lam)
        B_bar = ((Lam_bar - 1.0) / Lam)[:, None] * Bmat
        Bu = torch.einsum("bld,pd->blp", x + 0j, B_bar)
        h = torch.zeros_like(Bu[:, 0, :])
        hs = []
        for t in range(L):
            h = Lam_bar[None, :] * h + Bu[:, t, :]
            hs.append(h)
        hseq = torch.stack(hs, 1)
        y = 2.0 * torch.einsum("blp,dp->bld", hseq, Cmat).real + x * Dskip[None, None, :]
        return y

    def _B_ref(x, wqkv, w_phi, wo):
        B, S, D = x.shape
        H = 32; Dh = D // H
        qkv = (lambda x, *d: x.permute(*d))((x @ wqkv).reshape(B, S, 3, H, Dh), 2, 0, 3, 1, 4)
        q, k, v = qkv[0], qkv[1], qkv[2]
        pq = torch.einsum("bhsd,hdf->bhsf", q, w_phi)
        pk = torch.einsum("bhsd,hdf->bhsf", k, w_phi)
        phi_q = torch.concatenate([_softmax(pq, axis=-1), _softmax(-pq, axis=-1)], axis=-1)
        phi_k = torch.concatenate([_softmax(pk, axis=-1), _softmax(-pk, axis=-1)], axis=-1)
        kv = torch.einsum("bhsf,bhsd->bhfd", phi_k, v)
        z  = torch.einsum("bhsf->bhf", phi_k)
        num = torch.einsum("bhqf,bhfd->bhqd", phi_q, kv)
        den = torch.einsum("bhqf,bhf->bhq", phi_q, z)[..., None] + 1e-6
        o = num / den
        return (lambda x, *d: x.permute(*d))(o, 0, 2, 1, 3).reshape(B, S, D) @ wo

    a = _A_ref(A_x, A_Lambda_re, A_Lambda_im, A_B_re, A_B_im, A_C_re, A_C_im, A_log_dt, A_Dskip)
    b = _B_ref(B_x, B_wqkv, B_w_phi, B_wo)
    return torch.concatenate([a, b], axis=-1)
```

```cpp
#include <hip/hip_runtime.h>
#include <hip/hip_bf16.h>
typedef unsigned short u16;
__device__ __forceinline__ float bf2f(u16 v){ return __uint_as_float(((unsigned)v)<<16); }
__device__ __forceinline__ float rbf(float x){ return __bfloat162float(__float2bfloat16(x)); }
__global__ void k_u(const u16* x, const u16* Bre, const u16* Bim, float* ure, float* uim){
  int p = blockIdx.x*64+threadIdx.x; int l = blockIdx.y;
  float ar=0, ai=0;
  for(int d=0;d<512;d++){ float xv=bf2f(x[l*512+d]); ar+=xv*bf2f(Bre[p*512+d]); ai+=xv*bf2f(Bim[p*512+d]); }
  ure[l*512+p]=ar; uim[l*512+p]=ai;
}
__global__ void k_scan(const u16* Lre,const u16* Lim,const u16* ldt,float* ure,float* uim){
  int p = blockIdx.x*64+threadIdx.x;
  float dt = rbf(expf(bf2f(ldt[p])));
  float lr=bf2f(Lre[p]), li=bf2f(Lim[p]);
  float e=expf(dt*lr), c=cosf(dt*li), s=sinf(dt*li);
  float br=e*c, bi=e*s;
  float nr=br-1.f, ni=bi; float den=lr*lr+li*li;
  float cr=(nr*lr+ni*li)/den, ci=(ni*lr-nr*li)/den;
  float hr=0,hi=0;
  for(int l=0;l<2048;l++){
    float a=ure[l*512+p], b=uim[l*512+p];
    float xr=cr*a-ci*b, xi=cr*b+ci*a;
    float nhr=br*hr-bi*hi+xr, nhi=br*hi+bi*hr+xi;
    hr=nhr;hi=nhi;
    ure[l*512+p]=rbf(hr); uim[l*512+p]=rbf(hi);
  }
}
__global__ void k_y(const u16* x,const u16* Cre,const u16* Cim,const u16* D,const float* hre,const float* him,float* out){
  int d = blockIdx.x*64+threadIdx.x; int l = blockIdx.y;
  float a=0;
  for(int p=0;p<512;p++){ a+=hre[l*512+p]*bf2f(Cre[d*512+p])-him[l*512+p]*bf2f(Cim[d*512+p]); }
  out[l*1024+d]=2.f*a+rbf(bf2f(x[l*512+d])*bf2f(D[d]));
}
__global__ void k_qkv(const u16* x,const u16* w,float* qkv){
  int n = blockIdx.x*64+threadIdx.x; int l = blockIdx.y;
  float a=0;
  for(int d=0;d<512;d++) a+=bf2f(x[l*512+d])*bf2f(w[d*1536+n]);
  qkv[l*1536+n]=rbf(a);
}
__global__ void k_phi(const float* qkv,const u16* wphi,float* phi){
  int l=blockIdx.x; int t=threadIdx.x; int which=t>>5, h=t&31;
  float p[16]; float mx=-1e30f,mn=1e30f;
  #pragma unroll
  for(int f=0;f<16;f++){ float a=0; for(int d=0;d<16;d++) a+=qkv[l*1536+which*512+h*16+d]*bf2f(wphi[h*256+d*16+f]); p[f]=rbf(a); mx=fmaxf(mx,p[f]); mn=fminf(mn,p[f]); }
  float s1=0,s2=0; float e1[16],e2[16];
  #pragma unroll
  for(int f=0;f<16;f++){ e1[f]=expf(p[f]-mx); e2[f]=expf(mn-p[f]); s1+=e1[f]; s2+=e2[f]; }
  #pragma unroll
  for(int f=0;f<16;f++){ phi[((l*2+which)*32+h)*32+f]=rbf(e1[f]/s1); phi[((l*2+which)*32+h)*32+16+f]=rbf(e2[f]/s2); }
}
__global__ void k_kv(const float* qkv,const float* phi,float* kv){
  int h=blockIdx.x; int f=blockIdx.y; int d=threadIdx.x; if(d>=17) return;
  float a=0;
  for(int l=0;l<2048;l++){ float v = d<16? qkv[l*1536+1024+h*16+d]:1.f; a+=phi[((l*2+1)*32+h)*32+f]*v; }
  kv[(h*32+f)*17+d]=rbf(a);
}
__global__ void k_o(const float* phi,const float* kv,float* o){
  int l=blockIdx.x; int t=threadIdx.x; int h=t>>4,d=t&15;
  float num=0,den=0;
  for(int f=0;f<32;f++){ float q=phi[((l*2+0)*32+h)*32+f]; num+=q*kv[(h*32+f)*17+d]; den+=q*kv[(h*32+f)*17+16]; }
  num=rbf(num); den=rbf(rbf(den)+1e-6f);
  o[l*512+t]=rbf(num/den);
}
__global__ void k_out(const float* o,const u16* wo,float* out){
  int n = blockIdx.x*64+threadIdx.x; int l = blockIdx.y;
  float a=0;
  for(int d=0;d<512;d++) a+=o[l*512+d]*bf2f(wo[d*512+n]);
  out[l*1024+512+n]=rbf(a);
}
extern "C" void kernel_launch(void* const* d_in, const int* in_sizes, int n_in,
                              void* d_out, int out_size, void* d_ws, size_t ws_size,
                              hipStream_t stream) {
  const u16* Ax=(const u16*)d_in[0]; const u16* Lre=(const u16*)d_in[1]; const u16* Lim=(const u16*)d_in[2];
  const u16* Bre=(const u16*)d_in[3]; const u16* Bim=(const u16*)d_in[4]; const u16* Cre=(const u16*)d_in[5]; const u16* Cim=(const u16*)d_in[6];
  const u16* ldt=(const u16*)d_in[7]; const u16* D=(const u16*)d_in[8]; const u16* Bx=(const u16*)d_in[9];
  const u16* wqkv=(const u16*)d_in[10]; const u16* wphi=(const u16*)d_in[11]; const u16* wo=(const u16*)d_in[12];
  float* out=(float*)d_out; float* ws=(float*)d_ws;
  float* ure=ws; float* uim=ure+2048*512; float* qkv=uim+2048*512; float* phi=qkv+2048*1536; float* kv=phi+2048*2048; float* o=kv+32*32*17+64;
  k_u<<<dim3(8,2048),64,0,stream>>>(Ax,Bre,Bim,ure,uim);
  k_scan<<<8,64,0,stream>>>(Lre,Lim,ldt,ure,uim);
  k_y<<<dim3(8,2048),64,0,stream>>>(Ax,Cre,Cim,D,ure,uim,out);
  k_qkv<<<dim3(24,2048),64,0,stream>>>(Bx,wqkv,qkv);
  k_phi<<<2048,64,0,stream>>>(qkv,wphi,phi);
  k_kv<<<dim3(32,32),64,0,stream>>>(qkv,phi,kv);
  k_o<<<2048,512,0,stream>>>(phi,kv,o);
  k_out<<<dim3(8,2048),64,0,stream>>>(o,wo,out);
}
```

```cpp
#define MODE 1
#define ROT 0
#define PF 0
#define FIRST 0
#include <cstdio>
#include <hip/hip_runtime.h>
#include <hip/hip_bf16.h>
typedef unsigned short u16;
__device__ __forceinline__ float bf2f(u16 v){ return __uint_as_float(((unsigned)v)<<16); }
__device__ __forceinline__ float rbf(float x){ return __bfloat162float(__float2bfloat16(x)); }
__global__ void k_u(const u16* x, const u16* Bre, const u16* Bim, float* ure, float* uim){
  int p = blockIdx.x*64+threadIdx.x; int l = blockIdx.y;
  float ar=0, ai=0;
  for(int d=0;d<512;d++){ float xv=bf2f(x[l*512+d]); ar+=xv*bf2f(Bre[p*512+d]); ai+=xv*bf2f(Bim[p*512+d]); }
  ure[l*512+p]=ar; uim[l*512+p]=ai;
}
__global__ void k_scan(const u16* Lre,const u16* Lim,const u16* ldt,float* ure,float* uim){
  int p = blockIdx.x*64+threadIdx.x;
  float dt = rbf(expf(bf2f(ldt[p])));
  float lr=bf2f(Lre[p]), li=bf2f(Lim[p]);
  float e=expf(dt*lr), c=cosf(dt*li), s=sinf(dt*li);
  float br=e*c, bi=e*s;
  float nr=br-1.f, ni=bi; float den=lr*lr+li*li;
  float cr=(nr*lr+ni*li)/den, ci=(ni*lr-nr*li)/den;
  float hr=0,hi=0;
  for(int l=0;l<2048;l++){
    float a=ure[l*512+p], b=uim[l*512+p];
    float xr=cr*a-ci*b, xi=cr*b+ci*a;
    float nhr=br*hr-bi*hi+xr, nhi=br*hi+bi*hr+xi;
    hr=nhr;hi=nhi;
    ure[l*512+p]=rbf(hr); uim[l*512+p]=rbf(hi);
  }
}
__global__ void k_y(const u16* x,const u16* Cre,const u16* Cim,const u16* D,const float* hre,const float* him,float* out){
  int d = blockIdx.x*64+threadIdx.x; int l = blockIdx.y;
  float a=0;
  for(int p=0;p<512;p++){ a+=hre[l*512+p]*bf2f(Cre[d*512+p])-him[l*512+p]*bf2f(Cim[d*512+p]); }
  out[l*1024+d]=2.f*a+rbf(bf2f(x[l*512+d])*bf2f(D[d]));
}
__global__ void k_qkv(const u16* x,const u16* w,float* qkv){
  int n = blockIdx.x*64+threadIdx.x; int l = blockIdx.y;
  float a=0;
  for(int d=0;d<512;d++) a+=bf2f(x[l*512+d])*bf2f(w[d*1536+n]);
  qkv[l*1536+n]=rbf(a);
}
__global__ void k_phi(const float* qkv,const u16* wphi,float* phi){
  int l=blockIdx.x; int t=threadIdx.x; int which=t>>5, h=t&31;
  float p[16]; float mx=-1e30f,mn=1e30f;
  #pragma unroll
  for(int f=0;f<16;f++){ float a=0; for(int d=0;d<16;d++) a+=qkv[l*1536+which*512+h*16+d]*bf2f(wphi[h*256+d*16+f]); p[f]=rbf(a); mx=fmaxf(mx,p[f]); mn=fminf(mn,p[f]); }
  float s1=0,s2=0; float e1[16],e2[16];
  #pragma unroll
  for(int f=0;f<16;f++){ e1[f]=expf(p[f]-mx); e2[f]=expf(mn-p[f]); s1+=e1[f]; s2+=e2[f]; }
  #pragma unroll
  for(int f=0;f<16;f++){ phi[((l*2+which)*32+h)*32+f]=rbf(e1[f]/s1); phi[((l*2+which)*32+h)*32+16+f]=rbf(e2[f]/s2); }
}
__global__ void k_kv(const float* qkv,const float* phi,float* kv){
  int h=blockIdx.x; int f=blockIdx.y; int d=threadIdx.x; if(d>=17) return;
  float a=0;
  for(int l=0;l<2048;l++){ float v = d<16? qkv[l*1536+1024+h*16+d]:1.f; a+=phi[((l*2+1)*32+h)*32+f]*v; }
  kv[(h*32+f)*17+d]=rbf(a);
}
__global__ void k_o(const float* phi,const float* kv,float* o){
  int l=blockIdx.x; int t=threadIdx.x; int h=t>>4,d=t&15;
  float num=0,den=0;
  for(int f=0;f<32;f++){ float q=phi[((l*2+0)*32+h)*32+f]; num+=q*kv[(h*32+f)*17+d]; den+=q*kv[(h*32+f)*17+16]; }
  num=rbf(num); den=rbf(rbf(den)+1e-6f);
  o[l*512+t]=rbf(num/den);
}
__global__ void k_out(const float* o,const u16* wo,float* out){
  int n = blockIdx.x*64+threadIdx.x; int l = blockIdx.y;
  float a=0;
  for(int d=0;d<512;d++) a+=o[l*512+d]*bf2f(wo[d*512+n]);
  out[l*1024+512+n]=rbf(a);
}

typedef unsigned v4u __attribute__((ext_vector_type(4)));
typedef unsigned long long u64;
__device__ __forceinline__ u64 rt(){ u64 t; asm volatile("s_memrealtime %0\n\ts_waitcnt lgkmcnt(0)":"=s"(t)::"memory"); return t; }
__device__ __forceinline__ u64 ct(){ u64 t; asm volatile("s_memtime %0\n\ts_waitcnt lgkmcnt(0)":"=s"(t)::"memory"); return t; }
__device__ __forceinline__ void glds(const char* p, unsigned dst){
  unsigned keep;
  asm volatile("s_mov_b32 %0, m0\n\ts_mov_b32 m0, %2\n\ts_nop 0\n\tglobal_load_lds_dwordx4 %1, off\n\ts_mov_b32 m0, %0" : "=&s"(keep) : "v"(p), "s"(dst) : "memory");
}
#define R8(x) x x x x x x x x
#define NREC 24
__global__ __launch_bounds__(256,1) void diag(const char* X0, const char* W0a, const char* W0b, const char* X1, const char* W1, u64* rec, unsigned* sink){
  extern __shared__ __attribute__((aligned(16))) char lds[];
  u64 t0=rt(); u64 c0=ct();
  int b=blockIdx.x, xcd=b&7, j=b>>3;
  int wave=__builtin_amdgcn_readfirstlane(threadIdx.x>>6), lane=threadIdx.x&63;
  u64 s[NREC];
  #pragma unroll
  for(int i=0;i<NREC;i++) s[i]=0;
  int xi=j>>3, wi=j&7;
  int xt = ((xcd&3)*4 + xi);
  const char* tx = (xcd>=4? X1: X0) + (size_t)xt*131072;
  int k0 = ROT? ((wi+2*xi)&7) : 0;
  unsigned lb=(unsigned)(size_t)lds;
  #pragma unroll
  for(int kk=0;kk<8;kk++){
    int kb=(kk+k0)&7;
    #pragma unroll
    for(int i=0;i<4;i++){
      int rg=i*4+wave;
      const char* p = tx + (rg*8+(lane>>3))*1024 + kb*128 + (((lane&7)^((lane>>4)&3))*16);
      glds(p, lb + ((kk&3)*32+rg)*1024);
    }
    #pragma unroll
    for(int i=0;i<4;i++){
      int rg=i*4+wave;
      const char* p;
      if(xcd<4) p = (rg<8? W0a : W0b) + (size_t)(wi*64+(rg&7)*8+(lane>>3))*1024 + kb*128 + (((lane&7)^((lane>>4)&3))*16);
      else p = W1 + (size_t)(kb*64 + rg*4 + (lane>>4))*3072 + wi*256 + (lane&15)*16;
      glds(p, lb + ((kk&3)*32+16+rg)*1024);
    }
    s[kk]=ct()-c0;
    if(kk==0 && FIRST){ asm volatile("s_waitcnt vmcnt(7)":::"memory"); s[8]=ct()-c0; asm volatile("s_waitcnt vmcnt(0)":::"memory"); s[9]=ct()-c0; }
  }
  asm volatile("s_waitcnt vmcnt(8)":::"memory"); s[10]=ct()-c0;
  asm volatile("s_waitcnt vmcnt(0)":::"memory"); s[11]=ct()-c0;
  __builtin_amdgcn_s_barrier();
  u64 c3=ct(); u64 t1=rt();
  unsigned q=*(unsigned*)(lds+threadIdx.x*4);
  if(q==0x12345678) sink[0]=q;
  if(threadIdx.x==0){ rec[b*32+0]=t0; rec[b*32+1]=t1; rec[b*32+2]=c3-c0;
    #pragma unroll
    for(int i=0;i<NREC;i++) rec[b*32+4+i]=s[i]; }
}
__global__ void prn(u64* rec, u64* tmp){
  if(threadIdx.x!=0) return;
  u64 mn=~0ull,mxs=0,mxe=0;
  for(int b=0;b<256;b++){ u64 a=rec[b*32]; if(a<mn) mn=a; if(a>mxs) mxs=a; if(rec[b*32+1]>mxe) mxe=rec[b*32+1]; }
  int (*med)[18]=(int(*)[18])(tmp+1000);
  for(int half=0;half<2;half++)
  for(int f=0;f<17;f++){
    int n=0;
    for(int b=0;b<256;b++){ if(((b&7)>=4)!=half) continue; u64 v=(f==16)? rec[b*32+2] : rec[b*32+4+f]; int i=n++; while(i>0 && tmp[i-1]>v){ tmp[i]=tmp[i-1]; i--; } tmp[i]=v; }
    med[half][f]=(int)tmp[n/2]; if(f==16) med[half][17]=(int)tmp[n-1];
  }
  printf("D span %d skew %d | A tot %d max %d: %d %d %d %d %d %d %d %d | %d %d %d %d %d %d %d %d || B tot %d max %d: %d %d %d %d %d %d %d %d | %d %d %d %d\n",(int)(mxe-mn)*10,(int)(mxs-mn)*10,
    med[0][16],med[0][17],med[0][0],med[0][1],med[0][2],med[0][3],med[0][4],med[0][5],med[0][6],med[0][7],med[0][8],med[0][9],med[0][10],med[0][11],med[0][12],med[0][13],med[0][14],med[0][15],
    med[1][16],med[1][17],med[1][0],med[1][1],med[1][2],med[1][3],med[1][4],med[1][5],med[1][6],med[1][7],med[1][8],med[1][9],med[1][10],med[1][11]);
}
extern "C" void kernel_launch(void* const* d_in, const int* in_sizes, int n_in,
                              void* d_out, int out_size, void* d_ws, size_t ws_size,
                              hipStream_t stream) {
  const u16* Ax=(const u16*)d_in[0]; const u16* Lre=(const u16*)d_in[1]; const u16* Lim=(const u16*)d_in[2];
  const u16* Bre=(const u16*)d_in[3]; const u16* Bim=(const u16*)d_in[4]; const u16* Cre=(const u16*)d_in[5]; const u16* Cim=(const u16*)d_in[6];
  const u16* ldt=(const u16*)d_in[7]; const u16* D=(const u16*)d_in[8]; const u16* Bx=(const u16*)d_in[9];
  const u16* wqkv=(const u16*)d_in[10]; const u16* wphi=(const u16*)d_in[11]; const u16* wo=(const u16*)d_in[12];
  float* out=(float*)d_out; float* ws=(float*)d_ws;
  float* ure=ws; float* uim=ure+2048*512; float* qkv=uim+2048*512; float* phi=qkv+2048*1536; float* kv=phi+2048*2048; float* o=kv+32*32*17+64;
  u64* rec=(u64*)((char*)d_ws+100000000); hipFuncSetAttribute((const void*)diag,hipFuncAttributeMaxDynamicSharedMemorySize,132096);
  diag<<<256,256,132096,stream>>>((const char*)Ax,(const char*)Bre,(const char*)Bim,(const char*)Bx,(const char*)wqkv,rec,(unsigned*)(rec+20000));
  k_u<<<dim3(8,2048),64,0,stream>>>(Ax,Bre,Bim,ure,uim);
  k_scan<<<8,64,0,stream>>>(Lre,Lim,ldt,ure,uim);
  k_y<<<dim3(8,2048),64,0,stream>>>(Ax,Cre,Cim,D,ure,uim,out);
  k_qkv<<<dim3(24,2048),64,0,stream>>>(Bx,wqkv,qkv);
  k_phi<<<2048,64,0,stream>>>(qkv,wphi,phi);
  k_kv<<<dim3(32,32),64,0,stream>>>(qkv,phi,kv);
  k_o<<<2048,512,0,stream>>>(phi,kv,o);
  k_out<<<dim3(8,2048),64,0,stream>>>(o,wo,out);
  prn<<<1,64,0,stream>>>(rec,rec+10000);
}
```

```cpp
#include <hip/hip_runtime.h>
#pragma clang diagnostic ignored "-Winline-asm"

typedef unsigned short u16;
typedef unsigned int u32;
typedef unsigned long long u64;
typedef float f32x4 __attribute__((ext_vector_type(4)));
typedef u32 u32x4 __attribute__((ext_vector_type(4)));
typedef u32 u32x2 __attribute__((ext_vector_type(2)));
typedef __bf16 bf16x8 __attribute__((ext_vector_type(8)));
typedef short s16x4 __attribute__((ext_vector_type(4)));
typedef __amdgpu_buffer_rsrc_t rsrc_t;
#define LDS3 __attribute__((address_space(3)))
#define CST4 __attribute__((address_space(4)))
#define DEV __device__ __forceinline__

#define ROTA 1
#define ROTB 1
#define DEPA 5
#define DEPB 5

#define WS_EPA 0u
#define WS_EPB 256u
#define WS_FLA 4096u
#define WS_FLB 20480u
#define WS_GA 65536u
#define WS_GB 196608u
#define WS_H 1048576u
#define WS_O 5242880u
#define WS_SIZE 7340032u
#define WS_TS 8388608u
#define OOB 0xFFFFFF00u
#define SPIN_CAP (1 << 20)

struct Args {
  const u16* in[13];
  float* out;
  char* ws;
};

DEV u32 rfl(u32 x) { return __builtin_amdgcn_readfirstlane(x); }
DEV rsrc_t mkrsrc(const void* p, u32 bytes) { return __builtin_amdgcn_make_buffer_rsrc((void*)p, (short)0, (int)bytes, 0x00020000); }
DEV void glds(u32 voff, rsrc_t srd, u32 soff, u32 ldst) {
  asm volatile("s_mov_b32 m0, %0\n\ts_nop 0\n\tbuffer_load_dwordx4 %1, %2, %3 offen lds" ::"s"(ldst), "v"(voff), "s"(srd), "s"(soff) : "m0", "memory");
}
DEV void glds48(u32 voff, rsrc_t srd, u32 soff, u32 ldst) {
  asm volatile("s_mov_b32 m0, %0\n\ts_mov_b32 exec_hi, 0xffff\n\tbuffer_load_dwordx4 %1, %2, %3 offen lds\n\ts_mov_b32 exec_hi, -1" ::"s"(ldst), "v"(voff), "s"(srd), "s"(soff) : "m0", "memory");
}
template <int N> DEV void wait_bar() { asm volatile("s_waitcnt vmcnt(%0) lgkmcnt(0)\n\ts_barrier" ::"n"(N) : "memory"); }
DEV void lds_bar() { asm volatile("s_waitcnt lgkmcnt(0)\n\ts_barrier" ::: "memory"); }
DEV void raw_bar() { asm volatile("s_barrier" ::: "memory"); }
DEV void fence_c() { asm volatile("" ::: "memory"); }
DEV u32x4 lds128(u32 a) { return *reinterpret_cast<const LDS3 u32x4*>(a); }
DEV u32x2 lds64(u32 a) { return *reinterpret_cast<const LDS3 u32x2*>(a); }
DEV u32 lds16(u32 a) { return *reinterpret_cast<const LDS3 u16*>(a); }
DEV void sts128(u32 a, u32x4 v) { *reinterpret_cast<LDS3 u32x4*>(a) = v; }
DEV void sts64(u32 a, u32x2 v) { *reinterpret_cast<LDS3 u32x2*>(a) = v; }
DEV u32x2 ldstr(u32 a) { return __builtin_bit_cast(u32x2, __builtin_amdgcn_ds_read_tr16_b64_v4i16(reinterpret_cast<LDS3 s16x4*>(a))); }
DEV f32x4 mfma(u32x4 a, u32x4 b, f32x4 c) {
  return __builtin_amdgcn_mfma_f32_16x16x32_bf16(__builtin_bit_cast(bf16x8, a), __builtin_bit_cast(bf16x8, b), c, 0, 0, 0);
}
typedef float f32x2 __attribute__((ext_vector_type(2)));
typedef __bf16 bf16x2 __attribute__((ext_vector_type(2)));
DEV u32 cvtpk(float lo, float hi) { return __builtin_bit_cast(u32, __builtin_convertvector(f32x2{lo, hi}, bf16x2)); }
DEV float asf(u32 x) { return __uint_as_float(x); }
DEV u32 asu(float x) { return __float_as_uint(x); }
DEV float bflo(u32 x) { return asf(x << 16); }
DEV float bfhi(u32 x) { return asf(x & 0xffff0000u); }
DEV u32x4 cat(u32x2 a, u32x2 b) { u32x4 r; r.x = a.x; r.y = a.y; r.z = b.x; r.w = b.y; return r; }
DEV u32x4 ld_sc1(rsrc_t r, u32 voff, u32 soff) { return __builtin_amdgcn_raw_buffer_load_b128(r, voff, soff, 16); }
DEV u32x2 ld64_sc1(rsrc_t r, u32 voff, u32 soff) { return __builtin_amdgcn_raw_buffer_load_b64(r, voff, soff, 16); }
DEV u32x4 ld_pl(rsrc_t r, u32 voff, u32 soff) { return __builtin_amdgcn_raw_buffer_load_b128(r, voff, soff, 0); }
DEV void st_sc1(u32x4 v, rsrc_t r, u32 voff) { __builtin_amdgcn_raw_buffer_store_b128(v, r, voff, 0, 16); }
DEV void st32_sc1(u32 v, rsrc_t r, u32 voff, u32 soff) { __builtin_amdgcn_raw_buffer_store_b32(v, r, voff, soff, 16); }
#define SB() __builtin_amdgcn_sched_barrier(0)
template <class T> DEV void pin(T& x) { asm volatile("" : "+v"(x)); }
template <class T, class... R> DEV void pin(T& x, R&... r) { pin(x); pin(r...); }
DEV f32x2 pfma(f32x2 a, f32x2 b, f32x2 c) { return __builtin_elementwise_fma(a, b, c); }
DEV f32x2 bc(float x) { return f32x2{x, x}; }
DEV f32x2 lo2(f32x4 v) { return __builtin_shufflevector(v, v, 0, 1); }
DEV f32x2 hi2(f32x4 v) { return __builtin_shufflevector(v, v, 2, 3); }
DEV bool none(bool bad) { return __builtin_amdgcn_ballot_w64(bad) == 0; }
DEV float red32(float x) { auto r = __builtin_amdgcn_permlane32_swap(asu(x), asu(x), false, false); return asf(r[0]) + asf(r[1]); }
DEV float red16(float x) { auto r = __builtin_amdgcn_permlane16_swap(asu(x), asu(x), false, false); return asf(r[0]) + asf(r[1]); }
template <int CTRL> DEV u32 dpp(u32 x) { return __builtin_amdgcn_update_dpp(0u, x, CTRL, 0xf, 0xf, true); }
struct cf { float r, i; };
DEV void pinc(cf& x) { asm volatile("" : "+v"(x.r), "+v"(x.i)); }
DEV cf cmul(cf a, cf b) { cf o; o.r = a.r * b.r - a.i * b.i; o.i = a.r * b.i + a.i * b.r; return o; }
DEV cf cfma(cf a, cf b, cf c) {
  cf o; o.r = __builtin_fmaf(a.r, b.r, c.r); o.i = __builtin_fmaf(a.r, b.i, c.i);
  o.r = __builtin_fmaf(-a.i, b.i, o.r); o.i = __builtin_fmaf(a.i, b.r, o.i); return o;
}

#define TS_DECL
#define TS(i)
#define TS_END

#define A_TOP 131072u
#define A_EH (A_TOP + 1024u)

DEV cf csel(u32 g, cf a0, cf a1, cf a2, cf a3) {
  cf o;
  o.r = g == 0 ? a0.r : g == 1 ? a1.r : g == 2 ? a2.r : a3.r;
  o.i = g == 0 ? a0.i : g == 1 ? a1.i : g == 2 ? a2.i : a3.i;
  return o;
}

DEV void side_a(const Args& a, const u32 lane, const u32 w, const u32 chunk, const u32 s, const u32 k0) {
  TS_DECL
  const u32 g = lane >> 4, c = lane & 15;
  const u32 th = w >> 2, cg = w & 3;
  u32 p_lr, p_li, p_dt;
  u32x2 dsk[2];
  {
    const u32 o1 = (s * 64 + cg * 16 + c) * 2, o2 = s * 128 + th * 64 + g * 8;
    asm volatile("global_load_ushort %0, %1, %2" : "=v"(p_lr) : "v"(o1), "s"(a.in[1]) : "memory");
    asm volatile("global_load_ushort %0, %1, %2" : "=v"(p_li) : "v"(o1), "s"(a.in[2]) : "memory");
    asm volatile("global_load_ushort %0, %1, %2" : "=v"(p_dt) : "v"(o1), "s"(a.in[7]) : "memory");
    asm volatile("global_load_dwordx2 %0, %1, %2" : "=v"(dsk[0]) : "v"(o2), "s"(a.in[8]) : "memory");
    asm volatile("global_load_dwordx2 %0, %1, %2 offset:32" : "=v"(dsk[1]) : "v"(o2), "s"(a.in[8]) : "memory");
  }
  const rsrc_t rX = mkrsrc(a.in[0] + chunk * 65536u, 131072u);
  const rsrc_t rB = mkrsrc((w < 4 ? a.in[3] : a.in[4]) + s * 32768u, 65536u);
  const rsrc_t rCr = mkrsrc(a.in[5] + s * 32768u, 65536u);
  const rsrc_t rCi = mkrsrc(a.in[6] + s * 32768u, 65536u);
  const rsrc_t rW = mkrsrc(a.ws, WS_SIZE);
  u32 vx[2], vb[2];
#pragma unroll
  for (int i = 0; i < 2; i++) {
    const u32 rho = 8 * (2 * w + i) + (lane >> 3);
    const u32 unit = (lane & 7) ^ ((rho >> 1) & 7);
    const u32 tok = (rho & 64) + 16 * ((rho >> 2) & 3) + 4 * ((rho >> 4) & 3) + (rho & 3);
    vx[i] = tok * 1024 + unit * 16;
    vb[i] = (rho & 63) * 1024 + unit * 16;
  }
  const u32 vc = (8 * w + (lane >> 3)) * 1024 + (lane & 7) * 16;
  u32x4 cw[4][4];
  auto fetch = [&](int st, int i) {
    if (st < 8) {
      const u32 so = ((st + k0) & 7) * 128, d = (st & 3) * 32768 + w * 2048;
      if (i < 2) glds(vx[i], rX, so, d + i * 1024);
      else glds(vb[i - 2], rB, so, d + 16384 + (i - 2) * 1024);
    } else {
      cw[st - 8][i] = ld_pl((i & 1) ? rCi : rCr, vc, (st - 8) * 256 + (i >> 1) * 128);
    }
  };
#pragma unroll
  for (int st = 0; st < 3; st++)
#pragma unroll
    for (int i = 0; i < 4; i++) fetch(st, i);
  const u32 tag = *(const CST4 u32*)(a.ws + WS_EPA) + 1u;
  asm volatile("" ::"s"(tag));
  TS(2)
  asm volatile("s_waitcnt vmcnt(12)" : "+v"(p_lr), "+v"(p_li), "+v"(p_dt), "+v"(dsk[0]), "+v"(dsk[1])::"memory");
  cf lam, l2, lam16, lam64, c2, wg, wl[4];
  {
    const float L2E = 1.4426950408889634f;
    const float lr = bflo(p_lr), li = bflo(p_li);
    const float dt = bflo(cvtpk(__builtin_amdgcn_exp2f(bflo(p_dt) * L2E), 0.f));
    const float e = __builtin_amdgcn_exp2f(dt * lr * L2E);
    const float rev = __builtin_amdgcn_fractf(dt * li * 0.15915494309189535f);
    lam.r = e * __builtin_amdgcn_cosf(rev); lam.i = e * __builtin_amdgcn_sinf(rev);
    const float nr = lam.r - 1.f, ni = lam.i, rd = 2.f * __builtin_amdgcn_rcpf(lr * lr + li * li);
    c2.r = (nr * lr + ni * li) * rd; c2.i = (ni * lr - nr * li) * rd;
    l2 = cmul(lam, lam);
    const cf l4 = cmul(l2, l2), l8 = cmul(l4, l4);
    lam16 = cmul(l8, l8);
    const cf l32 = cmul(lam16, lam16), l48 = cmul(l32, lam16);
    lam64 = cmul(l32, l32);
    const cf mu = cmul(lam64, lam64), mu2 = cmul(mu, mu), mu3 = cmul(mu2, mu), mu4 = cmul(mu2, mu2);
    wg = csel(g, cf{1.f, 0.f}, lam16, l32, l48);
    wl[0] = csel(g, cf{1.f, 0.f}, mu, mu2, mu3);
    wl[1] = cmul(wl[0], mu4); wl[2] = cmul(wl[1], mu4); wl[3] = cmul(wl[2], mu4);
    pinc(lam); pinc(l2); pinc(lam16); pinc(lam64); pinc(c2); pinc(wg); pinc(wl[0]); pinc(wl[1]); pinc(wl[2]); pinc(wl[3]);
  }
  u32 vo[4], need = 0;
#pragma unroll
  for (int j = 0; j < 4; j++) {
    const int i = (int)chunk - 1 - (int)g - 4 * j;
    vo[j] = i >= 0 ? WS_GA + ((i * 8 + s) * 64 + cg * 16 + c) * 16 : OOB;
    need |= (i >= 0 ? 1u : 0u) << j;
  }
  u32 vga = WS_GA + ((chunk * 8 + s) * 64 + cg * 16 + c) * 16, aeh = A_EH + (cg * 16 + c) * 8;
  u32 vhs = WS_H + ((chunk * 8 + th * 4) * 32 + s * 4 + cg) * 1024 + g * 256 + c * 4;
  pin(vo[0], vo[1], vo[2], vo[3], need, vga, aeh, vhs);
  f32x4 ur[4], ui[4];
#pragma unroll
  for (int t = 0; t < 4; t++) { ur[t] = f32x4{0, 0, 0, 0}; ui[t] = f32x4{0, 0, 0, 0}; }
  const u32 ax0 = (th * 64 + c) * 128 + (g ^ (c >> 1)) * 16;
  const u32 ab0 = 16384 + (cg * 16 + c) * 128 + (g ^ (c >> 1)) * 16;
  const u32 st_skip = (s - k0) & 7;
  const u32 axs = (32 * cg + c) * 128 + ((4 * th + (g >> 1)) ^ (c >> 1)) * 16 + (g & 1) * 8;
  u32x2 xs[2][2];
#pragma unroll
  for (int i = 0; i < 2; i++) { xs[i][0] = u32x2{0, 0}; xs[i][1] = u32x2{0, 0}; }
  u32x4 f0[6], f1[6];
  auto rdf = [&](u32x4* f, int st, int kk) {
    const u32 sb = (st & 3) * 32768;
    f[4] = lds128((ab0 ^ (kk * 64)) + sb);
    f[0] = lds128((ax0 ^ (kk * 64)) + sb);
    f[5] = lds128((ab0 ^ (kk * 64)) + sb + 8192);
#pragma unroll
    for (int t = 1; t < 4; t++) f[t] = lds128((ax0 ^ (kk * 64)) + sb + t * 2048);
  };
  auto mm = [&](const u32x4* f, int nst, int i0) {
#pragma unroll
    for (int t = 0; t < 4; t++) {
      ur[t] = mfma(f[t], f[4], ur[t]);
      ui[t] = mfma(f[t], f[5], ui[t]);
      if (t == 0 || t == 2) { SB(); fetch(nst, i0 + (t >> 1)); SB(); }
    }
  };
#pragma unroll
  for (int st = 0; st < 8; st++) {
    SB();
    wait_bar<8>();
    if (st == 0) { TS(3) }
    if (st == 4) { TS(16) }
    if (st == 7) { TS(4) }
    rdf(f0, st, 0);
    SB();
    if (st > 0) mm(f1, st + 3, 0);
    else { fetch(3, 0); fetch(3, 1); }
    SB();
    rdf(f1, st, 1);
    if (st_skip == (u32)st) {
      const u32 sb = (st & 3) * 32768;
#pragma unroll
      for (int i = 0; i < 2; i++) { xs[i][0] = lds64(axs + sb + i * 2048); fence_c(); xs[i][1] = lds64((axs ^ 32) + sb + i * 2048); fence_c(); }
    }
    SB();
    mm(f0, st + 3, 2);
  }
  SB();
  mm(f1, 11, 0);
  SB();
  fetch(11, 2); fetch(11, 3);
  TS(5)
  cf e;
  {
    f32x2 er = lo2(ur[0]), ei = lo2(ui[0]);
#pragma unroll
    for (int k = 1; k < 8; k++) {
      const f32x2 R = (k & 1) ? hi2(ur[k >> 1]) : lo2(ur[k >> 1]), I = (k & 1) ? hi2(ui[k >> 1]) : lo2(ui[k >> 1]);
      const f32x2 nr = pfma(bc(-l2.i), ei, pfma(bc(l2.r), er, R));
      ei = pfma(bc(l2.i), er, pfma(bc(l2.r), ei, I));
      er = nr;
    }
    e = cfma(lam, cf{er.x, ei.x}, cf{er.y, ei.y});
  }
  cf eg[4];
  {
    auto r = __builtin_amdgcn_permlane32_swap(asu(e.r), asu(e.r), false, false);
    auto q = __builtin_amdgcn_permlane32_swap(asu(e.i), asu(e.i), false, false);
    auto r0 = __builtin_amdgcn_permlane16_swap(r[0], r[0], false, false);
    auto q0 = __builtin_amdgcn_permlane16_swap(q[0], q[0], false, false);
    auto r1 = __builtin_amdgcn_permlane16_swap(r[1], r[1], false, false);
    auto q1 = __builtin_amdgcn_permlane16_swap(q[1], q[1], false, false);
    eg[0].r = asf(r0[0]); eg[1].r = asf(r0[1]); eg[2].r = asf(r1[0]); eg[3].r = asf(r1[1]);
    eg[0].i = asf(q0[0]); eg[1].i = asf(q0[1]); eg[2].i = asf(q1[0]); eg[3].i = asf(q1[1]);
  }
  const cf s2 = cfma(lam16, eg[0], eg[1]), s3 = cfma(lam16, s2, eg[2]), eh = cfma(lam16, s3, eg[3]);
  if (th == 0 && g == 0) sts64(aeh, u32x2{asu(eh.r), asu(eh.i)});
  lds_bar();
  cf pre = csel(g, cf{0.f, 0.f}, eg[0], s2, s3);
  if (th == 1) {
    const u32x2 t = lds64(aeh);
    const cf e0{asf(t.x), asf(t.y)};
    const cf tot = cfma(lam64, e0, eh);
    if (g == 0) st_sc1(u32x4{asu(tot.r), tag, asu(tot.i), tag}, rW, vga);
    pre = cfma(wg, e0, pre);
    wg = cmul(wg, lam64);
  }
  TS(6)
  SB();
  f32x2 hr[8], hi[8], wr[8], wi[8];
  {
    const cf p2 = cmul(c2, pre);
    wg = cmul(c2, wg);
    cf h{p2.r, -p2.i};
    const cf lc{lam.r, -lam.i};
#pragma unroll
    for (int k = 0; k < 8; k++) {
      const f32x2 R = (k & 1) ? hi2(ur[k >> 1]) : lo2(ur[k >> 1]), I = (k & 1) ? hi2(ui[k >> 1]) : lo2(ui[k >> 1]);
      const f32x2 vr = pfma(bc(-c2.i), I, bc(c2.r) * R), vi = pfma(bc(-c2.i), R, bc(-c2.r) * I);
      const cf h0 = cfma(lc, h, cf{vr.x, vi.x});
      h = cfma(lc, h0, cf{vr.y, vi.y});
      hr[k] = f32x2{h0.r, h.r}; hi[k] = f32x2{h0.i, h.i};
    }
    wr[0] = f32x2{lam.r, l2.r}; wi[0] = f32x2{-lam.i, -l2.i};
#pragma unroll
    for (int k = 1; k < 8; k++) {
      wr[k] = pfma(bc(l2.i), wi[k - 1], bc(l2.r) * wr[k - 1]);
      wi[k] = pfma(bc(-l2.i), wr[k - 1], bc(l2.r) * wi[k - 1]);
    }
#pragma unroll
    for (int k = 0; k < 8; k++) pin(hr[k], hi[k], wr[k], wi[k]);
    pinc(wg);
  }
  SB();
  u32x4 gr[4];
#pragma unroll
  for (int j = 0; j < 4; j++) gr[j] = ld_sc1(rW, vo[j], 0);
  SB();
  TS(17)
  f32x4 acc[2][4];
#pragma unroll
  for (int i = 0; i < 2; i++) {
#pragma unroll
    for (int e2 = 0; e2 < 2; e2++) {
      const u32 p0 = cvtpk(bflo(xs[i][e2].x) * bflo(dsk[e2].x), bfhi(xs[i][e2].x) * bfhi(dsk[e2].x));
      const u32 p1 = cvtpk(bflo(xs[i][e2].y) * bflo(dsk[e2].y), bfhi(xs[i][e2].y) * bfhi(dsk[e2].y));
      acc[i][e2] = f32x4{bflo(p0), bfhi(p0), bflo(p1), bfhi(p1)};
      acc[i][2 + e2] = f32x4{0, 0, 0, 0};
      pin(acc[i][e2]);
    }
  }
  {
    const u32 u = lane & 7, pb = ((u >> 1) - 2 * s) & 15;
    const u32 rest = (w >> 1) * 1024 + (u & 1) * 512 + ((8 * (w & 1) + (lane >> 3)) ^ (2 * (pb & 3) + (u & 1))) * 16;
#pragma unroll
    for (int p = 0; p < 4; p++)
#pragma unroll
      for (int hf = 0; hf < 2; hf++) {
        const u32 ad = ((((pb + (p & 1) * 8 + hf * 4) & 15) << 12) | (rest ^ ((p >> 1) * 2048))) + (p >> 1) * 65536;
        const u32x4 R = cw[p][2 * hf], I = cw[p][2 * hf + 1];
        sts128(ad, u32x4{__builtin_amdgcn_perm(I.x, R.x, 0x05040100u), __builtin_amdgcn_perm(I.x, R.x, 0x07060302u),
                         __builtin_amdgcn_perm(I.y, R.y, 0x05040100u), __builtin_amdgcn_perm(I.y, R.y, 0x07060302u)});
        sts128(ad + 256, u32x4{__builtin_amdgcn_perm(I.z, R.z, 0x05040100u), __builtin_amdgcn_perm(I.z, R.z, 0x07060302u),
                               __builtin_amdgcn_perm(I.w, R.w, 0x05040100u), __builtin_amdgcn_perm(I.w, R.w, 0x07060302u)});
      }
  }
  SB();
  TS(18)
  {
#pragma unroll 1
    for (int it = 0; it < SPIN_CAP; it++) {
      u32 have = 0;
#pragma unroll
      for (int j = 0; j < 4; j++) have |= (((gr[j].y ^ tag) | (gr[j].w ^ tag)) == 0 ? 1u : 0u) << j;
      if (none((need & ~have) != 0)) break;
      fence_c();
#pragma unroll
      for (int j = 0; j < 4; j++) gr[j] = ld_sc1(rW, vo[j], 0);
    }
  }
  TS(7)
  {
    cf H = cmul(wl[0], cf{asf(gr[0].x), asf(gr[0].z)});
#pragma unroll
    for (int j = 1; j < 4; j++) H = cfma(wl[j], cf{asf(gr[j].x), asf(gr[j].z)}, H);
    H.r = red16(red32(H.r)); H.i = red16(red32(H.i));
    const cf z = cmul(wg, H);
#pragma unroll
    for (int k = 0; k < 8; k++) {
      const f32x2 yr = pfma(wi[k], bc(z.i), pfma(wr[k], bc(z.r), hr[k]));
      const f32x2 yi = pfma(wr[k], bc(-z.i), pfma(wi[k], bc(z.r), hi[k]));
      st32_sc1(cvtpk(yr.x, yi.x), rW, vhs + (k & 1) * 128, (k >> 1) * 32768);
      st32_sc1(cvtpk(yr.y, yi.y), rW, vhs + (k & 1) * 128 + 64, (k >> 1) * 32768);
    }
  }
  TS(8)
  wait_bar<0>();
  if (w == 0 && lane < 8) __builtin_amdgcn_raw_buffer_store_b64(u32x2{tag, tag}, rW, WS_FLA + (chunk * 8 + lane) * 128 + s * 8, 0, 16);
  TS(9)
  u32 cb[4];
#pragma unroll
  for (int i = 0; i < 4; i++) cb[i] = th * 65536 + g * 256 + ((c ^ (2 * i + (g >> 1))) * 16);
  u32x4 cfr[2][4];
  auto rdc = [&](int ii) {
#pragma unroll
    for (int ct = 0; ct < 4; ct++) cfr[ii & 1][ct] = lds128(cb[ii & 3] + ii * 4096 + ct * 1024);
  };
  rdc(0);
  {
    const u32 vf = WS_FLA + (chunk * 8 + s) * 128 + (th * 4 + (lane & 3)) * 8;
#pragma unroll 1
    for (int it = 0; it < SPIN_CAP; it++) {
      fence_c();
      const u32x2 f = ld64_sc1(rW, vf, 0);
      if (none(((f.x ^ tag) | (f.y ^ tag)) != 0)) break;
    }
  }
  TS(10)
  {
    u32x4 hf[16][2];
    const u32 vh = WS_H + ((chunk * 8 + 2 * cg) * 32 + th * 16) * 1024 + c * 64 + g * 16;
    auto ldh = [&](int ii, int i) { hf[ii][i] = ld_sc1(rW, vh, ((ii + 2 * s) & 15) * 1024 + i * 32768); };
#pragma unroll
    for (int ii = 0; ii < DEPA; ii++) { ldh(ii, 0); ldh(ii, 1); }
    SB();
    TS(11)
#pragma unroll
    for (int ii = 0; ii < 16; ii++) {
      if (ii < 15) rdc(ii + 1);
      SB();
#pragma unroll
      for (int ct = 0; ct < 4; ct++) {
        acc[0][ct] = mfma(cfr[ii & 1][ct], hf[ii][0], acc[0][ct]);
        acc[1][ct] = mfma(cfr[ii & 1][ct], hf[ii][1], acc[1][ct]);
        if ((ct == 0 || ct == 2) && ii + DEPA < 16) { SB(); ldh(ii + DEPA, ct >> 1); SB(); }
      }
      SB();
      if (ii == 0) { TS(12) }
    }
  }
  TS(13)
#pragma unroll
  for (int i = 0; i < 2; i++)
#pragma unroll
    for (int e2 = 0; e2 < 2; e2++) sts128(A_TOP + w * 4096 + (i * 2 + e2) * 1024 + lane * 16, __builtin_bit_cast(u32x4, acc[i][2 + e2]));
  lds_bar();
  TS(19)
  {
    float* op = a.out + (size_t)(chunk * 128 + (cg >> 1) * 64 + 16 * (c >> 2) + 8 * (cg & 1) + (c & 3)) * 1024 + s * 64 + th * 32 + 4 * g;
#pragma unroll
    for (int i = 0; i < 2; i++)
#pragma unroll
      for (int e2 = 0; e2 < 2; e2++) {
        const f32x4 o = acc[i][e2] + __builtin_bit_cast(f32x4, lds128(A_TOP + (w ^ 4) * 4096 + (i * 2 + e2) * 1024 + lane * 16));
        *(f32x4*)(op + i * 4096 + e2 * 16) = o;
      }
  }
  if (chunk == 15 && s == 0 && w == 0 && lane == 0) __builtin_amdgcn_raw_buffer_store_b32(tag, rW, WS_EPA, 0, 16);
  TS(14)
  TS_END
}

#define B_SLOT 45056u
#define B_WO 90112u
#define B_PHI 155648u
#define B_KVI (B_PHI + 1024u)
#define B_ZI (B_PHI + 3072u)

DEV void softmax2(const f32x4* p, u32* ph) {
  float mx = __builtin_fmaxf(p[0][0], p[0][1]), mn = __builtin_fminf(p[0][0], p[0][1]);
#pragma unroll
  for (int i = 2; i < 16; i += 2) {
    mx = __builtin_fmaxf(__builtin_fmaxf(mx, p[i >> 2][i & 3]), p[i >> 2][(i & 3) + 1]);
    mn = __builtin_fminf(__builtin_fminf(mn, p[i >> 2][i & 3]), p[i >> 2][(i & 3) + 1]);
  }
  const float L = 1.4426950408889634f;
  const float a = -mx * L, b = mn * L;
  f32x2 e1[8], e2[8];
#pragma unroll
  for (int i = 0; i < 8; i++) {
    const f32x2 v = (i & 1) ? hi2(p[i >> 1]) : lo2(p[i >> 1]);
    const f32x2 x1 = pfma(v, bc(L), bc(a)), x2 = pfma(v, bc(-L), bc(b));
    e1[i] = f32x2{__builtin_amdgcn_exp2f(x1.x), __builtin_amdgcn_exp2f(x1.y)};
    e2[i] = f32x2{__builtin_amdgcn_exp2f(x2.x), __builtin_amdgcn_exp2f(x2.y)};
  }
  const f32x2 s1 = ((e1[0] + e1[1]) + (e1[2] + e1[3])) + ((e1[4] + e1[5]) + (e1[6] + e1[7]));
  const f32x2 s2 = ((e2[0] + e2[1]) + (e2[2] + e2[3])) + ((e2[4] + e2[5]) + (e2[6] + e2[7]));
  const float r1 = __builtin_amdgcn_rcpf(s1.x + s1.y), r2 = __builtin_amdgcn_rcpf(s2.x + s2.y);
#pragma unroll
  for (int i = 0; i < 8; i++) {
    const f32x2 y1 = e1[i] * bc(r1), y2 = e2[i] * bc(r2);
    ph[i] = cvtpk(y1.x, y1.y);
    ph[8 + i] = cvtpk(y2.x, y2.y);
  }
}
DEV u32x4 pack_gran(f32x4 v, u32 t_lo, u32 t_hi) {
  u32x4 o;
  o.x = ((asu(v[0]) + 0x200u) & 0xFFFFFC00u) | t_lo;
  o.y = ((asu(v[1]) + 0x200u) & 0xFFFFFC00u) | t_hi;
  o.z = ((asu(v[2]) + 0x200u) & 0xFFFFFC00u) | t_lo;
  o.w = ((asu(v[3]) + 0x200u) & 0xFFFFFC00u) | t_hi;
  return o;
}
DEV u32 gran_bad(u32x4 v, u32 t_lo, u32 t_hi) { return (v.x ^ t_lo) | (v.y ^ t_hi) | (v.z ^ t_lo) | (v.w ^ t_hi); }
DEV f32x4 gran_val(u32x4 v) { return f32x4{asf(v.x & 0xFFFFFC00u), asf(v.y & 0xFFFFFC00u), asf(v.z & 0xFFFFFC00u), asf(v.w & 0xFFFFFC00u)}; }

DEV void side_b(const Args& a, const u32 lane, const u32 w, const u32 chunk, const u32 pair, const u32 tt, const u32 jc, const u32 k0) {
  TS_DECL
  const u32 g = lane >> 4, c = lane & 15;
  const u32 tg = w & 3, hh = w >> 2;
  const u32 rq = c >> 2, q = c & 3;
  const rsrc_t rX = mkrsrc(a.in[9] + chunk * 131072u, 262144u);
  const rsrc_t rQ = mkrsrc(a.in[10], 1572864u);
  const rsrc_t rP = mkrsrc(a.in[11] + pair * 512u, 1024u);
  const rsrc_t rO = mkrsrc(a.in[12], 524288u);
  const rsrc_t rW = mkrsrc(a.ws, WS_SIZE);
  u32 vx[2], vw[2], vo[2];
#pragma unroll
  for (int i = 0; i < 2; i++) {
    vx[i] = (32 * w + 8 * i + (lane >> 3)) * 1024 + ((lane & 7) ^ ((4 * i + (lane >> 4)) & 7)) * 16;
    const u32 U = 48 * (2 * w + i) + lane, k = (U >> 2) & 63;
    vw[i] = k * 3072 + (U >> 8) * 1024 + pair * 64 + ((U & 3) ^ ((k >> 2) & 2)) * 16;
    vo[i] = (64 * w + 8 * i + (lane >> 3)) * 1024 + jc * 128 + ((lane & 7) ^ (((lane >> 3) & 2) | (i << 2))) * 16;
  }
  asm volatile("s_nop 4");
  if (w == 0) glds(lane * 16, rP, 0, B_PHI);
  auto fetch = [&](int st, int i) {
    if (st < 8) {
      const u32 kb = (st + k0) & 7, d = (st % 3) * B_SLOT;
      if (i < 4) glds(vx[i & 1], rX, kb * 128 + (i >> 1) * 16384, d + w * 4096 + i * 1024);
      else glds48(vw[i - 4], rQ, kb * 196608, d + 32768 + w * 1536 + (i - 4) * 768);
    } else {
      const int n = (st - 8) * 6 + i;
      if (n < 8) glds(vo[n & 1], rO, (n >> 1) * 16384, B_WO + w * 8192 + n * 1024);
    }
  };
#pragma unroll
  for (int st = 0; st < 2; st++)
#pragma unroll
    for (int i = 0; i < 6; i++) fetch(st, i);
  const u32 tag = *(const CST4 u32*)(a.ws + WS_EPB) + 1u;
  asm volatile("" ::"s"(tag));
  TS(2)
  f32x4 qa[4], ka[4], va[4];
#pragma unroll
  for (int t = 0; t < 4; t++) { qa[t] = f32x4{0, 0, 0, 0}; ka[t] = f32x4{0, 0, 0, 0}; va[t] = f32x4{0, 0, 0, 0}; }
  const u32 ax0 = (tg * 64 + c) * 128 + (g ^ (c >> 1)) * 16;
  const u32 aw0 = 32768 + (8 * g + rq) * 64 + ((2 * hh + (q >> 1)) ^ ((g & 1) << 1)) * 16 + (q & 1) * 8;
  u32x4 aphi[4][2];
  u32x4 f0[7], f1[7];
  auto rdf = [&](u32x4* f, int st, int kk) {
    const u32 sb = (st % 3) * B_SLOT, aw = aw0 + sb + kk * 2048;
    f[5] = cat(ldstr(aw + 4096), ldstr(aw + 4096 + 256));
#pragma unroll
    for (int t = 0; t < 4; t++) f[t] = lds128((ax0 ^ (kk * 64)) + sb + t * 2048);
    f[6] = cat(ldstr(aw + 8192), ldstr(aw + 8192 + 256));
    f[4] = cat(ldstr(aw), ldstr(aw + 256));
  };
  auto mm = [&](const u32x4* f, int nst, int i0) {
#pragma unroll
    for (int t = 0; t < 4; t++) { ka[t] = mfma(f[5], f[t], ka[t]); if (t == 1) { SB(); fetch(nst, i0); SB(); } }
#pragma unroll
    for (int t = 0; t < 4; t++) { va[t] = mfma(f[t], f[6], va[t]); if (t == 1) { SB(); fetch(nst, i0 + 1); SB(); } }
#pragma unroll
    for (int t = 0; t < 4; t++) { qa[t] = mfma(f[4], f[t], qa[t]); if (t == 1) { SB(); fetch(nst, i0 + 2); SB(); } }
  };
#pragma unroll
  for (int st = 0; st < 8; st++) {
    SB();
    wait_bar<6>();
    if (st == 0) { TS(3) }
    if (st == 4) { TS(16) }
    if (st == 7) { TS(4) }
    rdf(f0, st, 0);
    SB();
    if (st > 0) mm(f1, st + 2, 0);
    else { fetch(2, 0); fetch(2, 1); fetch(2, 2); }
    SB();
    rdf(f1, st, 1);
    if (st == 0) {
#pragma unroll
      for (int i = 0; i < 4; i++) {
        const u32x2 wd = ldstr(B_PHI + hh * 512 + (4 * g + rq) * 32 + i * 8);
#pragma unroll
        for (int h = 0; h < 2; h++) {
          aphi[i][h].x = rq == 2 * h ? wd.x : 0u; aphi[i][h].y = rq == 2 * h ? wd.y : 0u;
          aphi[i][h].z = rq == 2 * h + 1 ? wd.x : 0u; aphi[i][h].w = rq == 2 * h + 1 ? wd.y : 0u;
          pin(aphi[i][h]);
        }
      }
    }
    SB();
    mm(f0, st + 2, 3);
  }
  SB();
#pragma unroll
  for (int t = 0; t < 4; t++) ka[t] = mfma(f1[5], f1[t], ka[t]);
#pragma unroll
  for (int t = 0; t < 4; t++) va[t] = mfma(f1[t], f1[6], va[t]);
#pragma unroll
  for (int t = 0; t < 4; t++) qa[t] = mfma(f1[4], f1[t], qa[t]);
  SB();
  TS(5)
  const u32 t_lo = tag & 0x3FFu, t_hi = (tag >> 10) & 0x3FFu;
  const f32x4 z4 = f32x4{0, 0, 0, 0};
  {
    const u32x4 b01 = u32x4{cvtpk(ka[0][0], ka[0][1]), cvtpk(ka[0][2], ka[0][3]), cvtpk(ka[1][0], ka[1][1]), cvtpk(ka[1][2], ka[1][3])};
    const u32x4 b23 = u32x4{cvtpk(ka[2][0], ka[2][1]), cvtpk(ka[2][2], ka[2][3]), cvtpk(ka[3][0], ka[3][1]), cvtpk(ka[3][2], ka[3][3])};
    f32x4 p[4];
#pragma unroll
    for (int i = 0; i < 4; i++) p[i] = mfma(aphi[i][0], b01, z4);
#pragma unroll
    for (int i = 0; i < 4; i++) p[i] = mfma(aphi[i][1], b23, p[i]);
    const u32x4 v01 = u32x4{cvtpk(va[0][0], va[0][1]), cvtpk(va[0][2], va[0][3]), cvtpk(va[1][0], va[1][1]), cvtpk(va[1][2], va[1][3])};
    const u32x4 v23 = u32x4{cvtpk(va[2][0], va[2][1]), cvtpk(va[2][2], va[2][3]), cvtpk(va[3][0], va[3][1]), cvtpk(va[3][2], va[3][3])};
    u32 ph[16];
    softmax2(p, ph);
    const u32 wa = w * 4096 + lane * 64, sw = (lane >> 1) & 3;
#pragma unroll
    for (int u = 0; u < 4; u++) sts128(wa + ((u ^ sw) * 16), u32x4{ph[4 * u], ph[4 * u + 1], ph[4 * u + 2], ph[4 * u + 3]});
    const u32 ra = w * 4096 + (4 * g + rq) * 64 + (((q >> 1) ^ ((2 * g + (rq >> 1)) & 3)) * 16) + (q & 1) * 8;
    const u32x4 ones = u32x4{0x3F803F80u, 0x3F803F80u, 0x3F803F80u, 0x3F803F80u};
    f32x4 kv[2], zz[2];
#pragma unroll
    for (int F = 0; F < 2; F++) {
      const u32x4 a01 = cat(ldstr((ra ^ (F * 32))), ldstr((ra ^ (F * 32)) + 1024));
      const u32x4 a23 = cat(ldstr((ra ^ (F * 32)) + 2048), ldstr((ra ^ (F * 32)) + 3072));
      kv[F] = mfma(a01, v01, z4); zz[F] = mfma(a01, ones, z4);
      kv[F] = mfma(a23, v23, kv[F]); zz[F] = mfma(a23, ones, zz[F]);
    }
#pragma unroll
    for (int F = 0; F < 2; F++) {
      sts128(w * 4096 + F * 1024 + lane * 16, __builtin_bit_cast(u32x4, kv[F]));
      if (c == 0) sts128(w * 4096 + 2048 + (F * 4 + g) * 16, __builtin_bit_cast(u32x4, zz[F]));
    }
  }
  lds_bar();
  TS(6)
  if (w < 5) {
    const u32 ra = w < 4 ? (w >> 1) * 16384 + (w & 1) * 1024 + lane * 16 : ((lane >> 3) & 1) * 16384 + 2048 + (lane & 7) * 16;
    const f32x4 sum = (__builtin_bit_cast(f32x4, lds128(ra)) + __builtin_bit_cast(f32x4, lds128(ra + 4096))) +
                      (__builtin_bit_cast(f32x4, lds128(ra + 8192)) + __builtin_bit_cast(f32x4, lds128(ra + 12288)));
    const u32 vo2 = (w < 4 || lane < 16) ? WS_GB + (pair * 8 + chunk) * 4352 + (w * 64 + lane) * 16 : OOB;
    st_sc1(pack_gran(sum, t_lo, t_hi), rW, vo2);
  }
  TS(7)
  u32x4 bq[4];
  {
    const u32x4 b01 = u32x4{cvtpk(qa[0][0], qa[0][1]), cvtpk(qa[0][2], qa[0][3]), cvtpk(qa[1][0], qa[1][1]), cvtpk(qa[1][2], qa[1][3])};
    const u32x4 b23 = u32x4{cvtpk(qa[2][0], qa[2][1]), cvtpk(qa[2][2], qa[2][3]), cvtpk(qa[3][0], qa[3][1]), cvtpk(qa[3][2], qa[3][3])};
    f32x4 p[4];
#pragma unroll
    for (int i = 0; i < 4; i++) p[i] = mfma(aphi[i][0], b01, z4);
#pragma unroll
    for (int i = 0; i < 4; i++) p[i] = mfma(aphi[i][1], b23, p[i]);
    u32 ph[16];
    softmax2(p, ph);
#pragma unroll
    for (int d = 0; d < 4; d++) {
      auto s0 = __builtin_amdgcn_permlane32_swap(ph[d], ph[8 + d], false, false);
      auto s1 = __builtin_amdgcn_permlane32_swap(ph[4 + d], ph[12 + d], false, false);
      auto t0 = __builtin_amdgcn_permlane16_swap(s0[0], s1[0], false, false);
      auto t1 = __builtin_amdgcn_permlane16_swap(s0[1], s1[1], false, false);
      bq[0][d] = t0[0]; bq[1][d] = t0[1]; bq[2][d] = t1[0]; bq[3][d] = t1[1];
    }
    pin(bq[0], bq[1], bq[2], bq[3]);
  }
  TS(8)
  {
    const u32 vg = WS_GB + (pair * 8 + (lane >> 5) * 4) * 4352 + (w * 32 + (lane & 31)) * 16;
    const u32 vz = tg == 0 ? WS_GB + (pair * 8 + (lane >> 3)) * 4352 + (256 + hh * 8 + (lane & 7)) * 16 : OOB;
    u32x4 gr[4], gz;
    const u32 zmask = tg == 0 ? 0x3FFu : 0u;
#pragma unroll 1
    for (int it = 0; it < SPIN_CAP; it++) {
      fence_c();
#pragma unroll
      for (int j = 0; j < 4; j++) gr[j] = ld_sc1(rW, vg, j * 4352);
      gz = ld_sc1(rW, vz, 0);
      const u32 bad = (((gran_bad(gr[0], t_lo, t_hi) | gran_bad(gr[1], t_lo, t_hi)) | (gran_bad(gr[2], t_lo, t_hi) | gran_bad(gr[3], t_lo, t_hi))) & 0x3FFu) |
                      (gran_bad(gz, t_lo, t_hi) & zmask);
      if (none(bad != 0)) break;
    }
    TS(9)
    f32x4 sum = (gran_val(gr[0]) + gran_val(gr[1])) + (gran_val(gr[2]) + gran_val(gr[3]));
#pragma unroll
    for (int i = 0; i < 4; i++) sum[i] = red32(sum[i]);
    if (lane < 32) sts64(B_KVI + hh * 1024 + c * 64 + ((w >> 1) & 1) * 32 + (2 * (w & 1) + g) * 8, u32x2{cvtpk(sum[0], sum[1]), cvtpk(sum[2], sum[3])});
    if (tg == 0) {
      f32x4 z = gran_val(gz);
#pragma unroll
      for (int i = 0; i < 4; i++) { z[i] += asf(dpp<0x128>(asu(z[i]))); z[i] = red32(red16(z[i])); }
      if (lane < 8) sts64(B_ZI + hh * 64 + lane * 8, u32x2{cvtpk(z[0], z[1]), cvtpk(z[2], z[3])});
    }
  }
  lds_bar();
  {
    const u32x4 akv = lds128(B_KVI + hh * 1024 + c * 64 + g * 16);
    const u32x4 az = lds128(B_ZI + hh * 64 + g * 16);
    u32 X[4][2];
#pragma unroll
    for (int t = 0; t < 4; t++) {
      const f32x4 num = mfma(akv, bq[t], z4), den = mfma(az, bq[t], z4);
      const float r = __builtin_amdgcn_rcpf(den[0] + 1e-6f);
      X[t][0] = cvtpk(num[0] * r, num[1] * r); X[t][1] = cvtpk(num[2] * r, num[3] * r);
    }
    const u32 vs = WS_O + ((chunk * 16 + tg * 4 + (g & 1)) * 16 + pair) * 1024 + (2 * hh + (g >> 1)) * 256 + c * 16;
#pragma unroll
    for (int tp = 0; tp < 2; tp++) {
      auto s0 = __builtin_amdgcn_permlane16_swap(X[2 * tp][0], X[2 * tp + 1][0], false, false);
      auto s1 = __builtin_amdgcn_permlane16_swap(X[2 * tp][1], X[2 * tp + 1][1], false, false);
      st_sc1(u32x4{s0[0], s1[0], s0[1], s1[1]}, rW, vs + tp * 32768);
    }
  }
  TS(10)
  wait_bar<0>();
  if (w == 0 && lane < 16) __builtin_amdgcn_raw_buffer_store_b64(u32x2{tag, tag}, rW, WS_FLB + (chunk * 16 + lane) * 128 + pair * 8, 0, 16);
  TS(11)
  const u32 wb = B_WO + hh * 32768 + (8 * g + rq) * 128 + (((q >> 1) ^ ((rq & 2) | ((g & 1) << 2))) * 16) + (q & 1) * 8;
  u32x4 wfr[2][4];
  auto rdw = [&](int ii) {
    const u32 y = ((ii + jc) & 7) * 4096;
#pragma unroll
    for (int ct = 0; ct < 4; ct++) {
      const u32 ad = (wb ^ ((ct ^ (2 * hh)) * 32)) + y;
      wfr[ii & 1][ct] = cat(ldstr(ad), ldstr(ad + 512));
    }
  };
  rdw(0);
  {
    const u32 vf = WS_FLB + (tt * 8 + jc) * 128 + (hh * 8 + (lane & 7)) * 8;
#pragma unroll 1
    for (int it = 0; it < SPIN_CAP; it++) {
      fence_c();
      const u32x2 f = ld64_sc1(rW, vf, 0);
      if (none(((f.x ^ tag) | (f.y ^ tag)) != 0)) break;
    }
  }
  TS(12)
  f32x4 acc[2][4];
#pragma unroll
  for (int i = 0; i < 2; i++)
#pragma unroll
    for (int ct = 0; ct < 4; ct++) acc[i][ct] = z4;
  {
    u32x4 of[8][2];
    const u32 vh = WS_O + ((tt * 8 + 2 * tg) * 16 + hh * 8) * 1024 + lane * 16;
    auto ldo = [&](int ii, int i) { of[ii][i] = ld_sc1(rW, vh, ((ii + jc) & 7) * 1024 + i * 16384); };
#pragma unroll
    for (int ii = 0; ii < DEPB; ii++) { ldo(ii, 0); ldo(ii, 1); }
    SB();
#pragma unroll
    for (int ii = 0; ii < 8; ii++) {
      if (ii < 7) rdw(ii + 1);
      SB();
#pragma unroll
      for (int ct = 0; ct < 4; ct++) {
        acc[0][ct] = mfma(wfr[ii & 1][ct], of[ii][0], acc[0][ct]);
        acc[1][ct] = mfma(wfr[ii & 1][ct], of[ii][1], acc[1][ct]);
        if ((ct == 0 || ct == 2) && ii + DEPB < 8) { SB(); ldo(ii + DEPB, ct >> 1); SB(); }
      }
      SB();
      if (ii == 0) { TS(13) }
    }
  }
  TS(14)
#pragma unroll
  for (int i = 0; i < 2; i++)
#pragma unroll
    for (int e2 = 0; e2 < 2; e2++) sts128(w * 4096 + (i * 2 + e2) * 1024 + lane * 16, __builtin_bit_cast(u32x4, acc[i][2 + e2]));
  lds_bar();
  {
    float* op = a.out + (size_t)(tt * 128 + tg * 32 + c) * 1024 + 512 + jc * 64 + hh * 32 + 4 * g;
#pragma unroll
    for (int i = 0; i < 2; i++)
#pragma unroll
      for (int e2 = 0; e2 < 2; e2++) {
        const f32x4 o = acc[i][e2] + __builtin_bit_cast(f32x4, lds128((w ^ 4) * 4096 + (i * 2 + e2) * 1024 + lane * 16));
        const u32 p0 = cvtpk(o[0], o[1]), p1 = cvtpk(o[2], o[3]);
        *(f32x4*)(op + i * 16384 + e2 * 16) = f32x4{bflo(p0), bfhi(p0), bflo(p1), bfhi(p1)};
      }
  }
  if (tt == 0 && jc == 0 && w == 0 && lane == 0) __builtin_amdgcn_raw_buffer_store_b32(tag, rW, WS_EPB, 0, 16);
  TS(15)
  TS_END
}

__global__ __launch_bounds__(512, 2) void fused_s5_hedgehog(const Args a) {
  __shared__ __attribute__((aligned(1024))) char smem[163840];
  asm volatile("" ::"s"((u32)(size_t)(LDS3 char*)smem) : "memory");
  const u32 b = blockIdx.x, xcd = b & 7, j = b >> 3;
  const u32 lane = threadIdx.x & 63, w = rfl(threadIdx.x >> 6);
  if (xcd < 4) {
    const u32 k0 = ROTA == 0 ? 0u : ROTA == 1 ? 4u * ((j ^ (j >> 3)) & 1) : ((j + 2 * (j >> 3)) & 7);
    side_a(a, lane, w, xcd * 4 + (j >> 3), j & 7, k0);
  } else {
    const u32 x = xcd - 4;
    const u32 k0 = ROTB == 0 ? 0u : ROTB == 1 ? 4u * ((j ^ (j >> 3)) & 1) : ((j + 2 * (j >> 3)) & 7);
    side_b(a, lane, w, (x & 1) * 4 + (j >> 3), (x >> 1) * 8 + (j & 7), x * 4 + (j >> 3), j & 7, k0);
  }
}

extern "C" void kernel_launch(void* const* d_in, const int* in_sizes, int n_in, void* d_out, int out_size, void* d_ws,
                              size_t ws_size, hipStream_t stream) {
  Args a;
  for (int i = 0; i < 13; i++) a.in[i] = (const u16*)d_in[i];
  a.out = (float*)d_out;
  a.ws = (char*)d_ws;
  fused_s5_hedgehog<<<256, 512, 0, stream>>>(a);
}
```

```cpp
#define TSTAMP
#define K1 0
#define I1 3
#define K2 1
#define I2 3
#include <hip/hip_runtime.h>
#pragma clang diagnostic ignored "-Winline-asm"

typedef unsigned short u16;
typedef unsigned int u32;
typedef unsigned long long u64;
typedef float f32x4 __attribute__((ext_vector_type(4)));
typedef u32 u32x4 __attribute__((ext_vector_type(4)));
typedef u32 u32x2 __attribute__((ext_vector_type(2)));
typedef __bf16 bf16x8 __attribute__((ext_vector_type(8)));
typedef short s16x4 __attribute__((ext_vector_type(4)));
typedef __amdgpu_buffer_rsrc_t rsrc_t;
#define LDS3 __attribute__((address_space(3)))
#define CST4 __attribute__((address_space(4)))
#define DEV __device__ __forceinline__

#define ROTA 1
#define ROTB 1
#define DEPA 5
#define DEPB 5

#define WS_EPA 0u
#define WS_EPB 256u
#define WS_FLA 4096u
#define WS_FLB 20480u
#define WS_GA 65536u
#define WS_GB 196608u
#define WS_H 1048576u
#define WS_O 5242880u
#define WS_SIZE 7340032u
#define WS_TS 8388608u
#define OOB 0xFFFFFF00u
#define SPIN_CAP (1 << 20)

struct Args {
  const u16* in[13];
  float* out;
  char* ws;
};

DEV u32 rfl(u32 x) { return __builtin_amdgcn_readfirstlane(x); }
DEV rsrc_t mkrsrc(const void* p, u32 bytes) { return __builtin_amdgcn_make_buffer_rsrc((void*)p, (short)0, (int)bytes, 0x00020000); }
DEV void glds(u32 voff, rsrc_t srd, u32 soff, u32 ldst) {
  asm volatile("s_mov_b32 m0, %0\n\ts_nop 0\n\tbuffer_load_dwordx4 %1, %2, %3 offen lds" ::"s"(ldst), "v"(voff), "s"(srd), "s"(soff) : "m0", "memory");
}
DEV void glds48(u32 voff, rsrc_t srd, u32 soff, u32 ldst) {
  asm volatile("s_mov_b32 m0, %0\n\ts_mov_b32 exec_hi, 0xffff\n\tbuffer_load_dwordx4 %1, %2, %3 offen lds\n\ts_mov_b32 exec_hi, -1" ::"s"(ldst), "v"(voff), "s"(srd), "s"(soff) : "m0", "memory");
}
template <int N> DEV void wait_bar() { asm volatile("s_waitcnt vmcnt(%0) lgkmcnt(0)\n\ts_barrier" ::"n"(N) : "memory"); }
DEV void lds_bar() { asm volatile("s_waitcnt lgkmcnt(0)\n\ts_barrier" ::: "memory"); }
DEV void raw_bar() { asm volatile("s_barrier" ::: "memory"); }
DEV void fence_c() { asm volatile("" ::: "memory"); }
DEV u32x4 lds128(u32 a) { return *reinterpret_cast<const LDS3 u32x4*>(a); }
DEV u32x2 lds64(u32 a) { return *reinterpret_cast<const LDS3 u32x2*>(a); }
DEV u32 lds16(u32 a) { return *reinterpret_cast<const LDS3 u16*>(a); }
DEV void sts128(u32 a, u32x4 v) { *reinterpret_cast<LDS3 u32x4*>(a) = v; }
DEV void sts64(u32 a, u32x2 v) { *reinterpret_cast<LDS3 u32x2*>(a) = v; }
DEV u32x2 ldstr(u32 a) { return __builtin_bit_cast(u32x2, __builtin_amdgcn_ds_read_tr16_b64_v4i16(reinterpret_cast<LDS3 s16x4*>(a))); }
DEV f32x4 mfma(u32x4 a, u32x4 b, f32x4 c) {
  return __builtin_amdgcn_mfma_f32_16x16x32_bf16(__builtin_bit_cast(bf16x8, a), __builtin_bit_cast(bf16x8, b), c, 0, 0, 0);
}
typedef float f32x2 __attribute__((ext_vector_type(2)));
typedef __bf16 bf16x2 __attribute__((ext_vector_type(2)));
DEV u32 cvtpk(float lo, float hi) { return __builtin_bit_cast(u32, __builtin_convertvector(f32x2{lo, hi}, bf16x2)); }
DEV float asf(u32 x) { return __uint_as_float(x); }
DEV u32 asu(float x) { return __float_as_uint(x); }
DEV float bflo(u32 x) { return asf(x << 16); }
DEV float bfhi(u32 x) { return asf(x & 0xffff0000u); }
DEV u32x4 cat(u32x2 a, u32x2 b) { u32x4 r; r.x = a.x; r.y = a.y; r.z = b.x; r.w = b.y; return r; }
DEV u32x4 ld_sc1(rsrc_t r, u32 voff, u32 soff) { return __builtin_amdgcn_raw_buffer_load_b128(r, voff, soff, 16); }
DEV u32x2 ld64_sc1(rsrc_t r, u32 voff, u32 soff) { return __builtin_amdgcn_raw_buffer_load_b64(r, voff, soff, 16); }
DEV u32x4 ld_pl(rsrc_t r, u32 voff, u32 soff) { return __builtin_amdgcn_raw_buffer_load_b128(r, voff, soff, 0); }
DEV void st_sc1(u32x4 v, rsrc_t r, u32 voff) { __builtin_amdgcn_raw_buffer_store_b128(v, r, voff, 0, 16); }
DEV void st32_sc1(u32 v, rsrc_t r, u32 voff, u32 soff) { __builtin_amdgcn_raw_buffer_store_b32(v, r, voff, soff, 16); }
#define SB() __builtin_amdgcn_sched_barrier(0)
template <class T> DEV void pin(T& x) { asm volatile("" : "+v"(x)); }
template <class T, class... R> DEV void pin(T& x, R&... r) { pin(x); pin(r...); }
DEV f32x2 pfma(f32x2 a, f32x2 b, f32x2 c) { return __builtin_elementwise_fma(a, b, c); }
DEV f32x2 bc(float x) { return f32x2{x, x}; }
DEV f32x2 lo2(f32x4 v) { return __builtin_shufflevector(v, v, 0, 1); }
DEV f32x2 hi2(f32x4 v) { return __builtin_shufflevector(v, v, 2, 3); }
DEV bool none(bool bad) { return __builtin_amdgcn_ballot_w64(bad) == 0; }
DEV float red32(float x) { auto r = __builtin_amdgcn_permlane32_swap(asu(x), asu(x), false, false); return asf(r[0]) + asf(r[1]); }
DEV float red16(float x) { auto r = __builtin_amdgcn_permlane16_swap(asu(x), asu(x), false, false); return asf(r[0]) + asf(r[1]); }
template <int CTRL> DEV u32 dpp(u32 x) { return __builtin_amdgcn_update_dpp(0u, x, CTRL, 0xf, 0xf, true); }
struct cf { float r, i; };
DEV void pinc(cf& x) { asm volatile("" : "+v"(x.r), "+v"(x.i)); }
DEV cf cmul(cf a, cf b) { cf o; o.r = a.r * b.r - a.i * b.i; o.i = a.r * b.i + a.i * b.r; return o; }
DEV cf cfma(cf a, cf b, cf c) {
  cf o; o.r = __builtin_fmaf(a.r, b.r, c.r); o.i = __builtin_fmaf(a.r, b.i, c.i);
  o.r = __builtin_fmaf(-a.i, b.i, o.r); o.i = __builtin_fmaf(a.i, b.r, o.i); return o;
}

#define NTS 24
#define TS_DECL u64 ts_[NTS]; _Pragma("unroll") for (int i_ = 0; i_ < NTS; i_++) ts_[i_] = 0; ts_[0] = rt_(); ts_[1] = ct_();
#define TS(i) ts_[i] = ct_();
#define TS_END                                                                                   \
  ts_[NTS - 1] = rt_();                                                                          \
  if (lane == 0) {                                                                               \
    u64* p_ = (u64*)(a.ws + WS_TS) + (size_t)(blockIdx.x * 8 + w) * NTS;                         \
    _Pragma("unroll") for (int i_ = 0; i_ < NTS; i_++) p_[i_] = ts_[i_];                         \
  }
DEV u64 rt_() { u64 t; asm volatile("s_memrealtime %0\n\ts_waitcnt lgkmcnt(0)" : "=s"(t)::"memory"); return t; }
DEV u64 ct_() { u64 t; asm volatile("s_memtime %0\n\ts_waitcnt lgkmcnt(0)" : "=s"(t)::"memory"); return t; }

#define A_TOP 131072u
#define A_EH (A_TOP + 1024u)

DEV cf csel(u32 g, cf a0, cf a1, cf a2, cf a3) {
  cf o;
  o.r = g == 0 ? a0.r : g == 1 ? a1.r : g == 2 ? a2.r : a3.r;
  o.i = g == 0 ? a0.i : g == 1 ? a1.i : g == 2 ? a2.i : a3.i;
  return o;
}

DEV void side_a(const Args& a, const u32 lane, const u32 w, const u32 chunk, const u32 s, const u32 k0) {
  TS_DECL
  const u32 g = lane >> 4, c = lane & 15;
  const u32 th = w >> 2, cg = w & 3;
  u32 p_lr, p_li, p_dt;
  u32x2 dsk[2];
  {
    const u32 o1 = (s * 64 + cg * 16 + c) * 2, o2 = s * 128 + th * 64 + g * 8;
    asm volatile("global_load_ushort %0, %1, %2" : "=v"(p_lr) : "v"(o1), "s"(a.in[1]) : "memory");
    asm volatile("global_load_ushort %0, %1, %2" : "=v"(p_li) : "v"(o1), "s"(a.in[2]) : "memory");
    asm volatile("global_load_ushort %0, %1, %2" : "=v"(p_dt) : "v"(o1), "s"(a.in[7]) : "memory");
    asm volatile("global_load_dwordx2 %0, %1, %2" : "=v"(dsk[0]) : "v"(o2), "s"(a.in[8]) : "memory");
    asm volatile("global_load_dwordx2 %0, %1, %2 offset:32" : "=v"(dsk[1]) : "v"(o2), "s"(a.in[8]) : "memory");
  }
  const rsrc_t rX = mkrsrc(a.in[0] + chunk * 65536u, 131072u);
  const rsrc_t rB = mkrsrc((w < 4 ? a.in[3] : a.in[4]) + s * 32768u, 65536u);
  const rsrc_t rCr = mkrsrc(a.in[5] + s * 32768u, 65536u);
  const rsrc_t rCi = mkrsrc(a.in[6] + s * 32768u, 65536u);
  const rsrc_t rW = mkrsrc(a.ws, WS_SIZE);
  u32 vx[2], vb[2];
#pragma unroll
  for (int i = 0; i < 2; i++) {
    const u32 rho = 8 * (2 * w + i) + (lane >> 3);
    const u32 unit = (lane & 7) ^ ((rho >> 1) & 7);
    const u32 tok = (rho & 64) + 16 * ((rho >> 2) & 3) + 4 * ((rho >> 4) & 3) + (rho & 3);
    vx[i] = tok * 1024 + unit * 16;
    vb[i] = (rho & 63) * 1024 + unit * 16;
  }
  const u32 vc = (8 * w + (lane >> 3)) * 1024 + (lane & 7) * 16;
  u32x4 cw[4][4];
  auto fetch = [&](int st, int i) {
    if (st < 8) {
      const u32 so = ((st + k0) & 7) * 128, d = (st & 3) * 32768 + w * 2048;
      if (i < 2) glds(vx[i], rX, so, d + i * 1024);
      else glds(vb[i - 2], rB, so, d + 16384 + (i - 2) * 1024);
    } else {
      cw[st - 8][i] = ld_pl((i & 1) ? rCi : rCr, vc, (st - 8) * 256 + (i >> 1) * 128);
    }
  };
#pragma unroll
  for (int st = 0; st < 3; st++)
#pragma unroll
    for (int i = 0; i < 4; i++) fetch(st, i);
  const u32 tag = *(const CST4 u32*)(a.ws + WS_EPA) + 1u;
  asm volatile("" ::"s"(tag));
  TS(2)
  asm volatile("s_waitcnt vmcnt(12)" : "+v"(p_lr), "+v"(p_li), "+v"(p_dt), "+v"(dsk[0]), "+v"(dsk[1])::"memory");
  cf lam, l2, lam16, lam64, c2, wg, wl[4];
  {
    const float L2E = 1.4426950408889634f;
    const float lr = bflo(p_lr), li = bflo(p_li);
    const float dt = bflo(cvtpk(__builtin_amdgcn_exp2f(bflo(p_dt) * L2E), 0.f));
    const float e = __builtin_amdgcn_exp2f(dt * lr * L2E);
    const float rev = __builtin_amdgcn_fractf(dt * li * 0.15915494309189535f);
    lam.r = e * __builtin_amdgcn_cosf(rev); lam.i = e * __builtin_amdgcn_sinf(rev);
    const float nr = lam.r - 1.f, ni = lam.i, rd = 2.f * __builtin_amdgcn_rcpf(lr * lr + li * li);
    c2.r = (nr * lr + ni * li) * rd; c2.i = (ni * lr - nr * li) * rd;
    l2 = cmul(lam, lam);
    const cf l4 = cmul(l2, l2), l8 = cmul(l4, l4);
    lam16 = cmul(l8, l8);
    const cf l32 = cmul(lam16, lam16), l48 = cmul(l32, lam16);
    lam64 = cmul(l32, l32);
    const cf mu = cmul(lam64, lam64), mu2 = cmul(mu, mu), mu3 = cmul(mu2, mu), mu4 = cmul(mu2, mu2);
    wg = csel(g, cf{1.f, 0.f}, lam16, l32, l48);
    wl[0] = csel(g, cf{1.f, 0.f}, mu, mu2, mu3);
    wl[1] = cmul(wl[0], mu4); wl[2] = cmul(wl[1], mu4); wl[3] = cmul(wl[2], mu4);
    pinc(lam); pinc(l2); pinc(lam16); pinc(lam64); pinc(c2); pinc(wg); pinc(wl[0]); pinc(wl[1]); pinc(wl[2]); pinc(wl[3]);
  }
  u32 vo[4], need = 0;
#pragma unroll
  for (int j = 0; j < 4; j++) {
    const int i = (int)chunk - 1 - (int)g - 4 * j;
    vo[j] = i >= 0 ? WS_GA + ((i * 8 + s) * 64 + cg * 16 + c) * 16 : OOB;
    need |= (i >= 0 ? 1u : 0u) << j;
  }
  u32 vga = WS_GA + ((chunk * 8 + s) * 64 + cg * 16 + c) * 16, aeh = A_EH + (cg * 16 + c) * 8;
  u32 vhs = WS_H + ((chunk * 8 + th * 4) * 32 + s * 4 + cg) * 1024 + g * 256 + c * 4;
  pin(vo[0], vo[1], vo[2], vo[3], need, vga, aeh, vhs);
  f32x4 ur[4], ui[4];
#pragma unroll
  for (int t = 0; t < 4; t++) { ur[t] = f32x4{0, 0, 0, 0}; ui[t] = f32x4{0, 0, 0, 0}; }
  const u32 ax0 = (th * 64 + c) * 128 + (g ^ (c >> 1)) * 16;
  const u32 ab0 = 16384 + (cg * 16 + c) * 128 + (g ^ (c >> 1)) * 16;
  const u32 st_skip = (s - k0) & 7;
  const u32 axs = (32 * cg + c) * 128 + ((4 * th + (g >> 1)) ^ (c >> 1)) * 16 + (g & 1) * 8;
  u32x2 xs[2][2];
#pragma unroll
  for (int i = 0; i < 2; i++) { xs[i][0] = u32x2{0, 0}; xs[i][1] = u32x2{0, 0}; }
  u32x4 f0[6], f1[6];
  auto rdf = [&](u32x4* f, int st, int kk) {
    const u32 sb = (st & 3) * 32768;
    f[4] = lds128((ab0 ^ (kk * 64)) + sb);
    f[0] = lds128((ax0 ^ (kk * 64)) + sb);
    f[5] = lds128((ab0 ^ (kk * 64)) + sb + 8192);
#pragma unroll
    for (int t = 1; t < 4; t++) f[t] = lds128((ax0 ^ (kk * 64)) + sb + t * 2048);
  };
  auto mm = [&](const u32x4* f, int nst, int i0) {
#pragma unroll
    for (int t = 0; t < 4; t++) {
      ur[t] = mfma(f[t], f[4], ur[t]);
      ui[t] = mfma(f[t], f[5], ui[t]);
      if (t == 0 || t == 2) { SB(); fetch(nst, i0 + (t >> 1)); SB(); }
    }
  };
#pragma unroll
  for (int st = 0; st < 8; st++) {
    SB();
    wait_bar<8>();
    if (st == 0) { TS(3) }
    if (st == 4) { TS(16) }
    if (st == 7) { TS(4) }
    rdf(f0, st, 0);
    SB();
    if (st > 0) mm(f1, st + 3, 0);
    else { fetch(3, 0); fetch(3, 1); }
    SB();
    rdf(f1, st, 1);
    if (st_skip == (u32)st) {
      const u32 sb = (st & 3) * 32768;
#pragma unroll
      for (int i = 0; i < 2; i++) { xs[i][0] = lds64(axs + sb + i * 2048); fence_c(); xs[i][1] = lds64((axs ^ 32) + sb + i * 2048); fence_c(); }
    }
    SB();
    mm(f0, st + 3, 2);
  }
  SB();
  mm(f1, 11, 0);
  SB();
  fetch(11, 2); fetch(11, 3);
  TS(5)
  cf e;
  {
    f32x2 er = lo2(ur[0]), ei = lo2(ui[0]);
#pragma unroll
    for (int k = 1; k < 8; k++) {
      const f32x2 R = (k & 1) ? hi2(ur[k >> 1]) : lo2(ur[k >> 1]), I = (k & 1) ? hi2(ui[k >> 1]) : lo2(ui[k >> 1]);
      const f32x2 nr = pfma(bc(-l2.i), ei, pfma(bc(l2.r), er, R));
      ei = pfma(bc(l2.i), er, pfma(bc(l2.r), ei, I));
      er = nr;
    }
    e = cfma(lam, cf{er.x, ei.x}, cf{er.y, ei.y});
  }
  cf eg[4];
  {
    auto r = __builtin_amdgcn_permlane32_swap(asu(e.r), asu(e.r), false, false);
    auto q = __builtin_amdgcn_permlane32_swap(asu(e.i), asu(e.i), false, false);
    auto r0 = __builtin_amdgcn_permlane16_swap(r[0], r[0], false, false);
    auto q0 = __builtin_amdgcn_permlane16_swap(q[0], q[0], false, false);
    auto r1 = __builtin_amdgcn_permlane16_swap(r[1], r[1], false, false);
    auto q1 = __builtin_amdgcn_permlane16_swap(q[1], q[1], false, false);
    eg[0].r = asf(r0[0]); eg[1].r = asf(r0[1]); eg[2].r = asf(r1[0]); eg[3].r = asf(r1[1]);
    eg[0].i = asf(q0[0]); eg[1].i = asf(q0[1]); eg[2].i = asf(q1[0]); eg[3].i = asf(q1[1]);
  }
  const cf s2 = cfma(lam16, eg[0], eg[1]), s3 = cfma(lam16, s2, eg[2]), eh = cfma(lam16, s3, eg[3]);
  if (th == 0 && g == 0) sts64(aeh, u32x2{asu(eh.r), asu(eh.i)});
  lds_bar();
  cf pre = csel(g, cf{0.f, 0.f}, eg[0], s2, s3);
  if (th == 1) {
    const u32x2 t = lds64(aeh);
    const cf e0{asf(t.x), asf(t.y)};
    const cf tot = cfma(lam64, e0, eh);
    if (g == 0) st_sc1(u32x4{asu(tot.r), tag, asu(tot.i), tag}, rW, vga);
    pre = cfma(wg, e0, pre);
    wg = cmul(wg, lam64);
  }
  TS(6)
  SB();
  f32x2 hr[8], hi[8], wr[8], wi[8];
  {
    const cf p2 = cmul(c2, pre);
    wg = cmul(c2, wg);
    cf h{p2.r, -p2.i};
    const cf lc{lam.r, -lam.i};
#pragma unroll
    for (int k = 0; k < 8; k++) {
      const f32x2 R = (k & 1) ? hi2(ur[k >> 1]) : lo2(ur[k >> 1]), I = (k & 1) ? hi2(ui[k >> 1]) : lo2(ui[k >> 1]);
      const f32x2 vr = pfma(bc(-c2.i), I, bc(c2.r) * R), vi = pfma(bc(-c2.i), R, bc(-c2.r) * I);
      const cf h0 = cfma(lc, h, cf{vr.x, vi.x});
      h = cfma(lc, h0, cf{vr.y, vi.y});
      hr[k] = f32x2{h0.r, h.r}; hi[k] = f32x2{h0.i, h.i};
    }
    wr[0] = f32x2{lam.r, l2.r}; wi[0] = f32x2{-lam.i, -l2.i};
#pragma unroll
    for (int k = 1; k < 8; k++) {
      wr[k] = pfma(bc(l2.i), wi[k - 1], bc(l2.r) * wr[k - 1]);
      wi[k] = pfma(bc(-l2.i), wr[k - 1], bc(l2.r) * wi[k - 1]);
    }
#pragma unroll
    for (int k = 0; k < 8; k++) pin(hr[k], hi[k], wr[k], wi[k]);
    pinc(wg);
  }
  SB();
  u32x4 gr[4];
#pragma unroll
  for (int j = 0; j < 4; j++) gr[j] = ld_sc1(rW, vo[j], 0);
  SB();
  TS(17)
  f32x4 acc[2][4];
#pragma unroll
  for (int i = 0; i < 2; i++) {
#pragma unroll
    for (int e2 = 0; e2 < 2; e2++) {
      const u32 p0 = cvtpk(bflo(xs[i][e2].x) * bflo(dsk[e2].x), bfhi(xs[i][e2].x) * bfhi(dsk[e2].x));
      const u32 p1 = cvtpk(bflo(xs[i][e2].y) * bflo(dsk[e2].y), bfhi(xs[i][e2].y) * bfhi(dsk[e2].y));
      acc[i][e2] = f32x4{bflo(p0), bfhi(p0), bflo(p1), bfhi(p1)};
      acc[i][2 + e2] = f32x4{0, 0, 0, 0};
      pin(acc[i][e2]);
    }
  }
  {
    const u32 u = lane & 7, pb = ((u >> 1) - 2 * s) & 15;
    const u32 rest = (w >> 1) * 1024 + (u & 1) * 512 + ((8 * (w & 1) + (lane >> 3)) ^ (2 * (pb & 3) + (u & 1))) * 16;
#pragma unroll
    for (int p = 0; p < 4; p++)
#pragma unroll
      for (int hf = 0; hf < 2; hf++) {
        const u32 ad = ((((pb + (p & 1) * 8 + hf * 4) & 15) << 12) | (rest ^ ((p >> 1) * 2048))) + (p >> 1) * 65536;
        const u32x4 R = cw[p][2 * hf], I = cw[p][2 * hf + 1];
        sts128(ad, u32x4{__builtin_amdgcn_perm(I.x, R.x, 0x05040100u), __builtin_amdgcn_perm(I.x, R.x, 0x07060302u),
                         __builtin_amdgcn_perm(I.y, R.y, 0x05040100u), __builtin_amdgcn_perm(I.y, R.y, 0x07060302u)});
        sts128(ad + 256, u32x4{__builtin_amdgcn_perm(I.z, R.z, 0x05040100u), __builtin_amdgcn_perm(I.z, R.z, 0x07060302u),
                               __builtin_amdgcn_perm(I.w, R.w, 0x05040100u), __builtin_amdgcn_perm(I.w, R.w, 0x07060302u)});
      }
  }
  SB();
  TS(18)
  {
#pragma unroll 1
    for (int it = 0; it < SPIN_CAP; it++) {
      u32 have = 0;
#pragma unroll
      for (int j = 0; j < 4; j++) have |= (((gr[j].y ^ tag) | (gr[j].w ^ tag)) == 0 ? 1u : 0u) << j;
      if (none((need & ~have) != 0)) break;
      fence_c();
#pragma unroll
      for (int j = 0; j < 4; j++) gr[j] = ld_sc1(rW, vo[j], 0);
    }
  }
  TS(7)
  {
    cf H = cmul(wl[0], cf{asf(gr[0].x), asf(gr[0].z)});
#pragma unroll
    for (int j = 1; j < 4; j++) H = cfma(wl[j], cf{asf(gr[j].x), asf(gr[j].z)}, H);
    H.r = red16(red32(H.r)); H.i = red16(red32(H.i));
    const cf z = cmul(wg, H);
#pragma unroll
    for (int k = 0; k < 8; k++) {
      const f32x2 yr = pfma(wi[k], bc(z.i), pfma(wr[k], bc(z.r), hr[k]));
      const f32x2 yi = pfma(wr[k], bc(-z.i), pfma(wi[k], bc(z.r), hi[k]));
      st32_sc1(cvtpk(yr.x, yi.x), rW, vhs + (k & 1) * 128, (k >> 1) * 32768);
      st32_sc1(cvtpk(yr.y, yi.y), rW, vhs + (k & 1) * 128 + 64, (k >> 1) * 32768);
    }
  }
  TS(8)
  wait_bar<0>();
  if (w == 0 && lane < 8) __builtin_amdgcn_raw_buffer_store_b64(u32x2{tag, tag}, rW, WS_FLA + (chunk * 8 + lane) * 128 + s * 8, 0, 16);
  TS(9)
  u32 cb[4];
#pragma unroll
  for (int i = 0; i < 4; i++) cb[i] = th * 65536 + g * 256 + ((c ^ (2 * i + (g >> 1))) * 16);
  u32x4 cfr[2][4];
  auto rdc = [&](int ii) {
#pragma unroll
    for (int ct = 0; ct < 4; ct++) cfr[ii & 1][ct] = lds128(cb[ii & 3] + ii * 4096 + ct * 1024);
  };
  rdc(0);
  {
    const u32 vf = WS_FLA + (chunk * 8 + s) * 128 + (th * 4 + (lane & 3)) * 8;
#pragma unroll 1
    for (int it = 0; it < SPIN_CAP; it++) {
      fence_c();
      const u32x2 f = ld64_sc1(rW, vf, 0);
      if (none(((f.x ^ tag) | (f.y ^ tag)) != 0)) break;
    }
  }
  TS(10)
  {
    u32x4 hf[16][2];
    const u32 vh = WS_H + ((chunk * 8 + 2 * cg) * 32 + th * 16) * 1024 + c * 64 + g * 16;
    auto ldh = [&](int ii, int i) { hf[ii][i] = ld_sc1(rW, vh, ((ii + 2 * s) & 15) * 1024 + i * 32768); };
#pragma unroll
    for (int ii = 0; ii < DEPA; ii++) { ldh(ii, 0); ldh(ii, 1); }
    SB();
    TS(11)
#pragma unroll
    for (int ii = 0; ii < 16; ii++) {
      if (ii < 15) rdc(ii + 1);
      SB();
#pragma unroll
      for (int ct = 0; ct < 4; ct++) {
        acc[0][ct] = mfma(cfr[ii & 1][ct], hf[ii][0], acc[0][ct]);
        acc[1][ct] = mfma(cfr[ii & 1][ct], hf[ii][1], acc[1][ct]);
        if ((ct == 0 || ct == 2) && ii + DEPA < 16) { SB(); ldh(ii + DEPA, ct >> 1); SB(); }
      }
      SB();
      if (ii == 0) { TS(12) }
    }
  }
  TS(13)
#pragma unroll
  for (int i = 0; i < 2; i++)
#pragma unroll
    for (int e2 = 0; e2 < 2; e2++) sts128(A_TOP + w * 4096 + (i * 2 + e2) * 1024 + lane * 16, __builtin_bit_cast(u32x4, acc[i][2 + e2]));
  lds_bar();
  TS(19)
  {
    float* op = a.out + (size_t)(chunk * 128 + (cg >> 1) * 64 + 16 * (c >> 2) + 8 * (cg & 1) + (c & 3)) * 1024 + s * 64 + th * 32 + 4 * g;
#pragma unroll
    for (int i = 0; i < 2; i++)
#pragma unroll
      for (int e2 = 0; e2 < 2; e2++) {
        const f32x4 o = acc[i][e2] + __builtin_bit_cast(f32x4, lds128(A_TOP + (w ^ 4) * 4096 + (i * 2 + e2) * 1024 + lane * 16));
        *(f32x4*)(op + i * 4096 + e2 * 16) = o;
      }
  }
  if (chunk == 15 && s == 0 && w == 0 && lane == 0) __builtin_amdgcn_raw_buffer_store_b32(tag, rW, WS_EPA, 0, 16);
  TS(14)
  TS_END
}

#define B_SLOT 45056u
#define B_WO 90112u
#define B_PHI 155648u
#define B_KVI (B_PHI + 1024u)
#define B_ZI (B_PHI + 3072u)

DEV void softmax2(const f32x4* p, u32* ph) {
  float mx = __builtin_fmaxf(p[0][0], p[0][1]), mn = __builtin_fminf(p[0][0], p[0][1]);
#pragma unroll
  for (int i = 2; i < 16; i += 2) {
    mx = __builtin_fmaxf(__builtin_fmaxf(mx, p[i >> 2][i & 3]), p[i >> 2][(i & 3) + 1]);
    mn = __builtin_fminf(__builtin_fminf(mn, p[i >> 2][i & 3]), p[i >> 2][(i & 3) + 1]);
  }
  const float L = 1.4426950408889634f;
  const float a = -mx * L, b = mn * L;
  f32x2 e1[8], e2[8];
#pragma unroll
  for (int i = 0; i < 8; i++) {
    const f32x2 v = (i & 1) ? hi2(p[i >> 1]) : lo2(p[i >> 1]);
    const f32x2 x1 = pfma(v, bc(L), bc(a)), x2 = pfma(v, bc(-L), bc(b));
    e1[i] = f32x2{__builtin_amdgcn_exp2f(x1.x), __builtin_amdgcn_exp2f(x1.y)};
    e2[i] = f32x2{__builtin_amdgcn_exp2f(x2.x), __builtin_amdgcn_exp2f(x2.y)};
  }
  const f32x2 s1 = ((e1[0] + e1[1]) + (e1[2] + e1[3])) + ((e1[4] + e1[5]) + (e1[6] + e1[7]));
  const f32x2 s2 = ((e2[0] + e2[1]) + (e2[2] + e2[3])) + ((e2[4] + e2[5]) + (e2[6] + e2[7]));
  const float r1 = __builtin_amdgcn_rcpf(s1.x + s1.y), r2 = __builtin_amdgcn_rcpf(s2.x + s2.y);
#pragma unroll
  for (int i = 0; i < 8; i++) {
    const f32x2 y1 = e1[i] * bc(r1), y2 = e2[i] * bc(r2);
    ph[i] = cvtpk(y1.x, y1.y);
    ph[8 + i] = cvtpk(y2.x, y2.y);
  }
}
DEV u32x4 pack_gran(f32x4 v, u32 t_lo, u32 t_hi) {
  u32x4 o;
  o.x = ((asu(v[0]) + 0x200u) & 0xFFFFFC00u) | t_lo;
  o.y = ((asu(v[1]) + 0x200u) & 0xFFFFFC00u) | t_hi;
  o.z = ((asu(v[2]) + 0x200u) & 0xFFFFFC00u) | t_lo;
  o.w = ((asu(v[3]) + 0x200u) & 0xFFFFFC00u) | t_hi;
  return o;
}
DEV u32 gran_bad(u32x4 v, u32 t_lo, u32 t_hi) { return (v.x ^ t_lo) | (v.y ^ t_hi) | (v.z ^ t_lo) | (v.w ^ t_hi); }
DEV f32x4 gran_val(u32x4 v) { return f32x4{asf(v.x & 0xFFFFFC00u), asf(v.y & 0xFFFFFC00u), asf(v.z & 0xFFFFFC00u), asf(v.w & 0xFFFFFC00u)}; }

DEV void side_b(const Args& a, const u32 lane, const u32 w, const u32 chunk, const u32 pair, const u32 tt, const u32 jc, const u32 k0) {
  TS_DECL
  const u32 g = lane >> 4, c = lane & 15;
  const u32 tg = w & 3, hh = w >> 2;
  const u32 rq = c >> 2, q = c & 3;
  const rsrc_t rX = mkrsrc(a.in[9] + chunk * 131072u, 262144u);
  const rsrc_t rQ = mkrsrc(a.in[10], 1572864u);
  const rsrc_t rP = mkrsrc(a.in[11] + pair * 512u, 1024u);
  const rsrc_t rO = mkrsrc(a.in[12], 524288u);
  const rsrc_t rW = mkrsrc(a.ws, WS_SIZE);
  u32 vx[2], vw[2], vo[2];
#pragma unroll
  for (int i = 0; i < 2; i++) {
    vx[i] = (32 * w + 8 * i + (lane >> 3)) * 1024 + ((lane & 7) ^ ((4 * i + (lane >> 4)) & 7)) * 16;
    const u32 U = 48 * (2 * w + i) + lane, k = (U >> 2) & 63;
    vw[i] = k * 3072 + (U >> 8) * 1024 + pair * 64 + ((U & 3) ^ ((k >> 2) & 2)) * 16;
    vo[i] = (64 * w + 8 * i + (lane >> 3)) * 1024 + jc * 128 + ((lane & 7) ^ (((lane >> 3) & 2) | (i << 2))) * 16;
  }
  asm volatile("s_nop 4");
  if (w == 0) glds(lane * 16, rP, 0, B_PHI);
  auto fetch = [&](int st, int i) {
    if (st < 8) {
      const u32 kb = (st + k0) & 7, d = (st % 3) * B_SLOT;
      if (i < 4) glds(vx[i & 1], rX, kb * 128 + (i >> 1) * 16384, d + w * 4096 + i * 1024);
      else glds48(vw[i - 4], rQ, kb * 196608, d + 32768 + w * 1536 + (i - 4) * 768);
    } else {
      const int n = (st - 8) * 6 + i;
      if (n < 8) glds(vo[n & 1], rO, (n >> 1) * 16384, B_WO + w * 8192 + n * 1024);
    }
  };
#pragma unroll
  for (int st = 0; st < 2; st++)
#pragma unroll
    for (int i = 0; i < 6; i++) fetch(st, i);
  const u32 tag = *(const CST4 u32*)(a.ws + WS_EPB) + 1u;
  asm volatile("" ::"s"(tag));
  TS(2)
  f32x4 qa[4], ka[4], va[4];
#pragma unroll
  for (int t = 0; t < 4; t++) { qa[t] = f32x4{0, 0, 0, 0}; ka[t] = f32x4{0, 0, 0, 0}; va[t] = f32x4{0, 0, 0, 0}; }
  const u32 ax0 = (tg * 64 + c) * 128 + (g ^ (c >> 1)) * 16;
  const u32 aw0 = 32768 + (8 * g + rq) * 64 + ((2 * hh + (q >> 1)) ^ ((g & 1) << 1)) * 16 + (q & 1) * 8;
  u32x4 aphi[4][2];
  u32x4 f0[7], f1[7];
  auto rdf = [&](u32x4* f, int st, int kk) {
    const u32 sb = (st % 3) * B_SLOT, aw = aw0 + sb + kk * 2048;
    f[5] = cat(ldstr(aw + 4096), ldstr(aw + 4096 + 256));
#pragma unroll
    for (int t = 0; t < 4; t++) f[t] = lds128((ax0 ^ (kk * 64)) + sb + t * 2048);
    f[6] = cat(ldstr(aw + 8192), ldstr(aw + 8192 + 256));
    f[4] = cat(ldstr(aw), ldstr(aw + 256));
  };
  auto mm = [&](const u32x4* f, int nst, int i0) {
#pragma unroll
    for (int t = 0; t < 4; t++) { ka[t] = mfma(f[5], f[t], ka[t]); if (t == 1) { SB(); fetch(nst, i0); SB(); } }
#pragma unroll
    for (int t = 0; t < 4; t++) { va[t] = mfma(f[t], f[6], va[t]); if (t == 1) { SB(); fetch(nst, i0 + 1); SB(); } }
#pragma unroll
    for (int t = 0; t < 4; t++) { qa[t] = mfma(f[4], f[t], qa[t]); if (t == 1) { SB(); fetch(nst, i0 + 2); SB(); } }
  };
#pragma unroll
  for (int st = 0; st < 8; st++) {
    SB();
    wait_bar<6>();
    if (st == 0) { TS(3) }
    if (st == 4) { TS(16) }
    if (st == 7) { TS(4) }
    rdf(f0, st, 0);
    SB();
    if (st > 0) mm(f1, st + 2, 0);
    else { fetch(2, 0); fetch(2, 1); fetch(2, 2); }
    SB();
    rdf(f1, st, 1);
    if (st == 0) {
#pragma unroll
      for (int i = 0; i < 4; i++) {
        const u32x2 wd = ldstr(B_PHI + hh * 512 + (4 * g + rq) * 32 + i * 8);
#pragma unroll
        for (int h = 0; h < 2; h++) {
          aphi[i][h].x = rq == 2 * h ? wd.x : 0u; aphi[i][h].y = rq == 2 * h ? wd.y : 0u;
          aphi[i][h].z = rq == 2 * h + 1 ? wd.x : 0u; aphi[i][h].w = rq == 2 * h + 1 ? wd.y : 0u;
          pin(aphi[i][h]);
        }
      }
    }
    SB();
    mm(f0, st + 2, 3);
  }
  SB();
#pragma unroll
  for (int t = 0; t < 4; t++) ka[t] = mfma(f1[5], f1[t], ka[t]);
#pragma unroll
  for (int t = 0; t < 4; t++) va[t] = mfma(f1[t], f1[6], va[t]);
#pragma unroll
  for (int t = 0; t < 4; t++) qa[t] = mfma(f1[4], f1[t], qa[t]);
  SB();
  TS(5)
  const u32 t_lo = tag & 0x3FFu, t_hi = (tag >> 10) & 0x3FFu;
  const f32x4 z4 = f32x4{0, 0, 0, 0};
  {
    const u32x4 b01 = u32x4{cvtpk(ka[0][0], ka[0][1]), cvtpk(ka[0][2], ka[0][3]), cvtpk(ka[1][0], ka[1][1]), cvtpk(ka[1][2], ka[1][3])};
    const u32x4 b23 = u32x4{cvtpk(ka[2][0], ka[2][1]), cvtpk(ka[2][2], ka[2][3]), cvtpk(ka[3][0], ka[3][1]), cvtpk(ka[3][2], ka[3][3])};
    f32x4 p[4];
#pragma unroll
    for (int i = 0; i < 4; i++) p[i] = mfma(aphi[i][0], b01, z4);
#pragma unroll
    for (int i = 0; i < 4; i++) p[i] = mfma(aphi[i][1], b23, p[i]);
    const u32x4 v01 = u32x4{cvtpk(va[0][0], va[0][1]), cvtpk(va[0][2], va[0][3]), cvtpk(va[1][0], va[1][1]), cvtpk(va[1][2], va[1][3])};
    const u32x4 v23 = u32x4{cvtpk(va[2][0], va[2][1]), cvtpk(va[2][2], va[2][3]), cvtpk(va[3][0], va[3][1]), cvtpk(va[3][2], va[3][3])};
    u32 ph[16];
    softmax2(p, ph);
    const u32 wa = w * 4096 + lane * 64, sw = (lane >> 1) & 3;
#pragma unroll
    for (int u = 0; u < 4; u++) sts128(wa + ((u ^ sw) * 16), u32x4{ph[4 * u], ph[4 * u + 1], ph[4 * u + 2], ph[4 * u + 3]});
    const u32 ra = w * 4096 + (4 * g + rq) * 64 + (((q >> 1) ^ ((2 * g + (rq >> 1)) & 3)) * 16) + (q & 1) * 8;
    const u32x4 ones = u32x4{0x3F803F80u, 0x3F803F80u, 0x3F803F80u, 0x3F803F80u};
    f32x4 kv[2], zz[2];
#pragma unroll
    for (int F = 0; F < 2; F++) {
      const u32x4 a01 = cat(ldstr((ra ^ (F * 32))), ldstr((ra ^ (F * 32)) + 1024));
      const u32x4 a23 = cat(ldstr((ra ^ (F * 32)) + 2048), ldstr((ra ^ (F * 32)) + 3072));
      kv[F] = mfma(a01, v01, z4); zz[F] = mfma(a01, ones, z4);
      kv[F] = mfma(a23, v23, kv[F]); zz[F] = mfma(a23, ones, zz[F]);
    }
#pragma unroll
    for (int F = 0; F < 2; F++) {
      sts128(w * 4096 + F * 1024 + lane * 16, __builtin_bit_cast(u32x4, kv[F]));
      if (c == 0) sts128(w * 4096 + 2048 + (F * 4 + g) * 16, __builtin_bit_cast(u32x4, zz[F]));
    }
  }
  lds_bar();
  TS(6)
  if (w < 5) {
    const u32 ra = w < 4 ? (w >> 1) * 16384 + (w & 1) * 1024 + lane * 16 : ((lane >> 3) & 1) * 16384 + 2048 + (lane & 7) * 16;
    const f32x4 sum = (__builtin_bit_cast(f32x4, lds128(ra)) + __builtin_bit_cast(f32x4, lds128(ra + 4096))) +
                      (__builtin_bit_cast(f32x4, lds128(ra + 8192)) + __builtin_bit_cast(f32x4, lds128(ra + 12288)));
    const u32 vo2 = (w < 4 || lane < 16) ? WS_GB + (pair * 8 + chunk) * 4352 + (w * 64 + lane) * 16 : OOB;
    st_sc1(pack_gran(sum, t_lo, t_hi), rW, vo2);
  }
  TS(7)
  u32x4 bq[4];
  {
    const u32x4 b01 = u32x4{cvtpk(qa[0][0], qa[0][1]), cvtpk(qa[0][2], qa[0][3]), cvtpk(qa[1][0], qa[1][1]), cvtpk(qa[1][2], qa[1][3])};
    const u32x4 b23 = u32x4{cvtpk(qa[2][0], qa[2][1]), cvtpk(qa[2][2], qa[2][3]), cvtpk(qa[3][0], qa[3][1]), cvtpk(qa[3][2], qa[3][3])};
    f32x4 p[4];
#pragma unroll
    for (int i = 0; i < 4; i++) p[i] = mfma(aphi[i][0], b01, z4);
#pragma unroll
    for (int i = 0; i < 4; i++) p[i] = mfma(aphi[i][1], b23, p[i]);
    u32 ph[16];
    softmax2(p, ph);
#pragma unroll
    for (int d = 0; d < 4; d++) {
      auto s0 = __builtin_amdgcn_permlane32_swap(ph[d], ph[8 + d], false, false);
      auto s1 = __builtin_amdgcn_permlane32_swap(ph[4 + d], ph[12 + d], false, false);
      auto t0 = __builtin_amdgcn_permlane16_swap(s0[0], s1[0], false, false);
      auto t1 = __builtin_amdgcn_permlane16_swap(s0[1], s1[1], false, false);
      bq[0][d] = t0[0]; bq[1][d] = t0[1]; bq[2][d] = t1[0]; bq[3][d] = t1[1];
    }
    pin(bq[0], bq[1], bq[2], bq[3]);
  }
  TS(8)
  {
    const u32 vg = WS_GB + (pair * 8 + (lane >> 5) * 4) * 4352 + (w * 32 + (lane & 31)) * 16;
    const u32 vz = tg == 0 ? WS_GB + (pair * 8 + (lane >> 3)) * 4352 + (256 + hh * 8 + (lane & 7)) * 16 : OOB;
    u32x4 gr[4], gz;
    const u32 zmask = tg == 0 ? 0x3FFu : 0u;
#pragma unroll 1
    for (int it = 0; it < SPIN_CAP; it++) {
      fence_c();
#pragma unroll
      for (int j = 0; j < 4; j++) gr[j] = ld_sc1(rW, vg, j * 4352);
      gz = ld_sc1(rW, vz, 0);
      const u32 bad = (((gran_bad(gr[0], t_lo, t_hi) | gran_bad(gr[1], t_lo, t_hi)) | (gran_bad(gr[2], t_lo, t_hi) | gran_bad(gr[3], t_lo, t_hi))) & 0x3FFu) |
                      (gran_bad(gz, t_lo, t_hi) & zmask);
      if (none(bad != 0)) break;
    }
    TS(9)
    f32x4 sum = (gran_val(gr[0]) + gran_val(gr[1])) + (gran_val(gr[2]) + gran_val(gr[3]));
#pragma unroll
    for (int i = 0; i < 4; i++) sum[i] = red32(sum[i]);
    if (lane < 32) sts64(B_KVI + hh * 1024 + c * 64 + ((w >> 1) & 1) * 32 + (2 * (w & 1) + g) * 8, u32x2{cvtpk(sum[0], sum[1]), cvtpk(sum[2], sum[3])});
    if (tg == 0) {
      f32x4 z = gran_val(gz);
#pragma unroll
      for (int i = 0; i < 4; i++) { z[i] += asf(dpp<0x128>(asu(z[i]))); z[i] = red32(red16(z[i])); }
      if (lane < 8) sts64(B_ZI + hh * 64 + lane * 8, u32x2{cvtpk(z[0], z[1]), cvtpk(z[2], z[3])});
    }
  }
  lds_bar();
  {
    const u32x4 akv = lds128(B_KVI + hh * 1024 + c * 64 + g * 16);
    const u32x4 az = lds128(B_ZI + hh * 64 + g * 16);
    u32 X[4][2];
#pragma unroll
    for (int t = 0; t < 4; t++) {
      const f32x4 num = mfma(akv, bq[t], z4), den = mfma(az, bq[t], z4);
      const float r = __builtin_amdgcn_rcpf(den[0] + 1e-6f);
      X[t][0] = cvtpk(num[0] * r, num[1] * r); X[t][1] = cvtpk(num[2] * r, num[3] * r);
    }
    const u32 vs = WS_O + ((chunk * 16 + tg * 4 + (g & 1)) * 16 + pair) * 1024 + (2 * hh + (g >> 1)) * 256 + c * 16;
#pragma unroll
    for (int tp = 0; tp < 2; tp++) {
      auto s0 = __builtin_amdgcn_permlane16_swap(X[2 * tp][0], X[2 * tp + 1][0], false, false);
      auto s1 = __builtin_amdgcn_permlane16_swap(X[2 * tp][1], X[2 * tp + 1][1], false, false);
      st_sc1(u32x4{s0[0], s1[0], s0[1], s1[1]}, rW, vs + tp * 32768);
    }
  }
  TS(10)
  wait_bar<0>();
  if (w == 0 && lane < 16) __builtin_amdgcn_raw_buffer_store_b64(u32x2{tag, tag}, rW, WS_FLB + (chunk * 16 + lane) * 128 + pair * 8, 0, 16);
  TS(11)
  const u32 wb = B_WO + hh * 32768 + (8 * g + rq) * 128 + (((q >> 1) ^ ((rq & 2) | ((g & 1) << 2))) * 16) + (q & 1) * 8;
  u32x4 wfr[2][4];
  auto rdw = [&](int ii) {
    const u32 y = ((ii + jc) & 7) * 4096;
#pragma unroll
    for (int ct = 0; ct < 4; ct++) {
      const u32 ad = (wb ^ ((ct ^ (2 * hh)) * 32)) + y;
      wfr[ii & 1][ct] = cat(ldstr(ad), ldstr(ad + 512));
    }
  };
  rdw(0);
  {
    const u32 vf = WS_FLB + (tt * 8 + jc) * 128 + (hh * 8 + (lane & 7)) * 8;
#pragma unroll 1
    for (int it = 0; it < SPIN_CAP; it++) {
      fence_c();
      const u32x2 f = ld64_sc1(rW, vf, 0);
      if (none(((f.x ^ tag) | (f.y ^ tag)) != 0)) break;
    }
  }
  TS(12)
  f32x4 acc[2][4];
#pragma unroll
  for (int i = 0; i < 2; i++)
#pragma unroll
    for (int ct = 0; ct < 4; ct++) acc[i][ct] = z4;
  {
    u32x4 of[8][2];
    const u32 vh = WS_O + ((tt * 8 + 2 * tg) * 16 + hh * 8) * 1024 + lane * 16;
    auto ldo = [&](int ii, int i) { of[ii][i] = ld_sc1(rW, vh, ((ii + jc) & 7) * 1024 + i * 16384); };
#pragma unroll
    for (int ii = 0; ii < DEPB; ii++) { ldo(ii, 0); ldo(ii, 1); }
    SB();
#pragma unroll
    for (int ii = 0; ii < 8; ii++) {
      if (ii < 7) rdw(ii + 1);
      SB();
#pragma unroll
      for (int ct = 0; ct < 4; ct++) {
        acc[0][ct] = mfma(wfr[ii & 1][ct], of[ii][0], acc[0][ct]);
        acc[1][ct] = mfma(wfr[ii & 1][ct], of[ii][1], acc[1][ct]);
        if ((ct == 0 || ct == 2) && ii + DEPB < 8) { SB(); ldo(ii + DEPB, ct >> 1); SB(); }
      }
      SB();
      if (ii == 0) { TS(13) }
    }
  }
  TS(14)
#pragma unroll
  for (int i = 0; i < 2; i++)
#pragma unroll
    for (int e2 = 0; e2 < 2; e2++) sts128(w * 4096 + (i * 2 + e2) * 1024 + lane * 16, __builtin_bit_cast(u32x4, acc[i][2 + e2]));
  lds_bar();
  {
    float* op = a.out + (size_t)(tt * 128 + tg * 32 + c) * 1024 + 512 + jc * 64 + hh * 32 + 4 * g;
#pragma unroll
    for (int i = 0; i < 2; i++)
#pragma unroll
      for (int e2 = 0; e2 < 2; e2++) {
        const f32x4 o = acc[i][e2] + __builtin_bit_cast(f32x4, lds128((w ^ 4) * 4096 + (i * 2 + e2) * 1024 + lane * 16));
        const u32 p0 = cvtpk(o[0], o[1]), p1 = cvtpk(o[2], o[3]);
        *(f32x4*)(op + i * 16384 + e2 * 16) = f32x4{bflo(p0), bfhi(p0), bflo(p1), bfhi(p1)};
      }
  }
  if (tt == 0 && jc == 0 && w == 0 && lane == 0) __builtin_amdgcn_raw_buffer_store_b32(tag, rW, WS_EPB, 0, 16);
  TS(15)
  TS_END
}

__global__ __launch_bounds__(512, 2) void fused_s5_hedgehog(const Args a) {
  __shared__ __attribute__((aligned(1024))) char smem[163840];
  asm volatile("" ::"s"((u32)(size_t)(LDS3 char*)smem) : "memory");
  const u32 b = blockIdx.x, xcd = b & 7, j = b >> 3;
  const u32 lane = threadIdx.x & 63, w = rfl(threadIdx.x >> 6);
  if (xcd < 4) {
    const u32 k0 = ROTA == 0 ? 0u : ROTA == 1 ? 4u * ((j ^ (j >> 3)) & 1) : ((j + 2 * (j >> 3)) & 7);
    side_a(a, lane, w, xcd * 4 + (j >> 3), j & 7, k0);
  } else {
    const u32 x = xcd - 4;
    const u32 k0 = ROTB == 0 ? 0u : ROTB == 1 ? 4u * ((j ^ (j >> 3)) & 1) : ((j + 2 * (j >> 3)) & 7);
    side_b(a, lane, w, (x & 1) * 4 + (j >> 3), (x >> 1) * 8 + (j & 7), x * 4 + (j >> 3), j & 7, k0);
  }
}

__device__ u64 metric(const u64* ts, int kind, int idx, u64* sm){
  int t=threadIdx.x; u64 mn=~0ull, mxs=0, mxe=0, acc=0;
  for(int b=t;b<2048;b+=256){ mn=min(mn,ts[b*NTS]); mxs=max(mxs,ts[b*NTS]); mxe=max(mxe,ts[b*NTS+NTS-1]); }
  sm[t]=mn; sm[256+t]=mxs; sm[512+t]=mxe; __syncthreads();
  for(int i=0;i<256;i++){ mn=min(mn,sm[i]); mxs=max(mxs,sm[256+i]); mxe=max(mxe,sm[512+i]); }
  __syncthreads();
  for(int b=t;b<2048;b+=256){ bool A=((b>>3)&7)<4;
    if(kind==0&&A) acc+=ts[b*NTS+idx]-ts[b*NTS+1]; if(kind==1&&!A) acc+=ts[b*NTS+idx]-ts[b*NTS+1];
    if(kind==4&&A) acc+=ts[b*NTS+NTS-1]-mn; if(kind==5&&!A) acc+=ts[b*NTS+NTS-1]-mn; if(kind==6&&A) acc+=ts[b*NTS]-mn; if(kind==7&&!A) acc+=ts[b*NTS]-mn; }
  sm[t]=acc; __syncthreads(); acc=0; for(int i=0;i<256;i++) acc+=sm[i]; __syncthreads();
  if(kind<2) return (acc/1024+125)/250;
  if(kind==2) return ((mxe-mn)*8+50)/100; if(kind==3) return ((mxs-mn)*8+50)/100;
  return ((acc/128)+50)/100;
}
__global__ void prn(const char* ws){
  __shared__ u64 sm[768];
  const u64* ts=(const u64*)(ws+WS_TS);
  u64 a=min(metric(ts,K1,I1,sm),(u64)199), b=min(metric(ts,K2,I2,sm),(u64)199);
  if(threadIdx.x==0){ u64 v=(a*200+b)*400; u64 t0=rt_(); while(rt_()-t0<v) __builtin_amdgcn_s_sleep(8); }
}
extern "C" void kernel_launch(void* const* d_in, const int* in_sizes, int n_in, void* d_out, int out_size, void* d_ws,
                              size_t ws_size, hipStream_t stream) {
  Args a;
  for (int i = 0; i < 13; i++) a.in[i] = (const u16*)d_in[i];
  a.out = (float*)d_out;
  a.ws = (char*)d_ws;
  fused_s5_hedgehog<<<256, 512, 0, stream>>>(a);
  prn<<<1,256,0,stream>>>((const char*)d_ws);
}
```

```cpp
#define OAUX 16
#include <hip/hip_runtime.h>
#pragma clang diagnostic ignored "-Winline-asm"

typedef unsigned short u16;
typedef unsigned int u32;
typedef unsigned long long u64;
typedef float f32x4 __attribute__((ext_vector_type(4)));
typedef u32 u32x4 __attribute__((ext_vector_type(4)));
typedef u32 u32x2 __attribute__((ext_vector_type(2)));
typedef __bf16 bf16x8 __attribute__((ext_vector_type(8)));
typedef short s16x4 __attribute__((ext_vector_type(4)));
typedef __amdgpu_buffer_rsrc_t rsrc_t;
#define LDS3 __attribute__((address_space(3)))
#define CST4 __attribute__((address_space(4)))
#define DEV __device__ __forceinline__

#define ROTA 1
#define ROTB 1
#define DEPA 5
#define DEPB 5

#define WS_EPA 0u
#define WS_EPB 256u
#define WS_FLA 4096u
#define WS_FLB 20480u
#define WS_GA 65536u
#define WS_GB 196608u
#define WS_H 1048576u
#define WS_O 5242880u
#define WS_SIZE 7340032u
#define WS_TS 8388608u
#define OOB 0xFFFFFF00u
#define SPIN_CAP (1 << 20)

struct Args {
  const u16* in[13];
  float* out;
  char* ws;
};

DEV u32 rfl(u32 x) { return __builtin_amdgcn_readfirstlane(x); }
DEV rsrc_t mkrsrc(const void* p, u32 bytes) { return __builtin_amdgcn_make_buffer_rsrc((void*)p, (short)0, (int)bytes, 0x00020000); }
DEV void glds(u32 voff, rsrc_t srd, u32 soff, u32 ldst) {
  asm volatile("s_mov_b32 m0, %0\n\ts_nop 0\n\tbuffer_load_dwordx4 %1, %2, %3 offen lds" ::"s"(ldst), "v"(voff), "s"(srd), "s"(soff) : "m0", "memory");
}
DEV void glds48(u32 voff, rsrc_t srd, u32 soff, u32 ldst) {
  asm volatile("s_mov_b32 m0, %0\n\ts_mov_b32 exec_hi, 0xffff\n\tbuffer_load_dwordx4 %1, %2, %3 offen lds\n\ts_mov_b32 exec_hi, -1" ::"s"(ldst), "v"(voff), "s"(srd), "s"(soff) : "m0", "memory");
}
template <int N> DEV void wait_bar() { asm volatile("s_waitcnt vmcnt(%0) lgkmcnt(0)\n\ts_barrier" ::"n"(N) : "memory"); }
DEV void lds_bar() { asm volatile("s_waitcnt lgkmcnt(0)\n\ts_barrier" ::: "memory"); }
DEV void raw_bar() { asm volatile("s_barrier" ::: "memory"); }
DEV void fence_c() { asm volatile("" ::: "memory"); }
DEV u32x4 lds128(u32 a) { return *reinterpret_cast<const LDS3 u32x4*>(a); }
DEV u32x2 lds64(u32 a) { return *reinterpret_cast<const LDS3 u32x2*>(a); }
DEV u32 lds16(u32 a) { return *reinterpret_cast<const LDS3 u16*>(a); }
DEV void sts128(u32 a, u32x4 v) { *reinterpret_cast<LDS3 u32x4*>(a) = v; }
DEV void sts64(u32 a, u32x2 v) { *reinterpret_cast<LDS3 u32x2*>(a) = v; }
DEV u32x2 ldstr(u32 a) { return __builtin_bit_cast(u32x2, __builtin_amdgcn_ds_read_tr16_b64_v4i16(reinterpret_cast<LDS3 s16x4*>(a))); }
DEV f32x4 mfma(u32x4 a, u32x4 b, f32x4 c) {
  return __builtin_amdgcn_mfma_f32_16x16x32_bf16(__builtin_bit_cast(bf16x8, a), __builtin_bit_cast(bf16x8, b), c, 0, 0, 0);
}
typedef float f32x2 __attribute__((ext_vector_type(2)));
typedef __bf16 bf16x2 __attribute__((ext_vector_type(2)));
DEV u32 cvtpk(float lo, float hi) { return __builtin_bit_cast(u32, __builtin_convertvector(f32x2{lo, hi}, bf16x2)); }
DEV float asf(u32 x) { return __uint_as_float(x); }
DEV u32 asu(float x) { return __float_as_uint(x); }
DEV float bflo(u32 x) { return asf(x << 16); }
DEV float bfhi(u32 x) { return asf(x & 0xffff0000u); }
DEV u32x4 cat(u32x2 a, u32x2 b) { u32x4 r; r.x = a.x; r.y = a.y; r.z = b.x; r.w = b.y; return r; }
DEV u32x4 ld_sc1(rsrc_t r, u32 voff, u32 soff) { return __builtin_amdgcn_raw_buffer_load_b128(r, voff, soff, 16); }
DEV u32x2 ld64_sc1(rsrc_t r, u32 voff, u32 soff) { return __builtin_amdgcn_raw_buffer_load_b64(r, voff, soff, 16); }
DEV u32x4 ld_pl(rsrc_t r, u32 voff, u32 soff) { return __builtin_amdgcn_raw_buffer_load_b128(r, voff, soff, 0); }
DEV void st_sc1(u32x4 v, rsrc_t r, u32 voff) { __builtin_amdgcn_raw_buffer_store_b128(v, r, voff, 0, 16); }
DEV void st32_sc1(u32 v, rsrc_t r, u32 voff, u32 soff) { __builtin_amdgcn_raw_buffer_store_b32(v, r, voff, soff, 16); }
#define SB() __builtin_amdgcn_sched_barrier(0)
template <class T> DEV void pin(T& x) { asm volatile("" : "+v"(x)); }
template <class T, class... R> DEV void pin(T& x, R&... r) { pin(x); pin(r...); }
DEV f32x2 pfma(f32x2 a, f32x2 b, f32x2 c) { return __builtin_elementwise_fma(a, b, c); }
DEV f32x2 bc(float x) { return f32x2{x, x}; }
DEV f32x2 lo2(f32x4 v) { return __builtin_shufflevector(v, v, 0, 1); }
DEV f32x2 hi2(f32x4 v) { return __builtin_shufflevector(v, v, 2, 3); }
DEV bool none(bool bad) { return __builtin_amdgcn_ballot_w64(bad) == 0; }
DEV float red32(float x) { auto r = __builtin_amdgcn_permlane32_swap(asu(x), asu(x), false, false); return asf(r[0]) + asf(r[1]); }
DEV float red16(float x) { auto r = __builtin_amdgcn_permlane16_swap(asu(x), asu(x), false, false); return asf(r[0]) + asf(r[1]); }
template <int CTRL> DEV u32 dpp(u32 x) { return __builtin_amdgcn_update_dpp(0u, x, CTRL, 0xf, 0xf, true); }
struct cf { float r, i; };
DEV void pinc(cf& x) { asm volatile("" : "+v"(x.r), "+v"(x.i)); }
DEV cf cmul(cf a, cf b) { cf o; o.r = a.r * b.r - a.i * b.i; o.i = a.r * b.i + a.i * b.r; return o; }
DEV cf cfma(cf a, cf b, cf c) {
  cf o; o.r = __builtin_fmaf(a.r, b.r, c.r); o.i = __builtin_fmaf(a.r, b.i, c.i);
  o.r = __builtin_fmaf(-a.i, b.i, o.r); o.i = __builtin_fmaf(a.i, b.r, o.i); return o;
}

#define TS_DECL
#define TS(i)
#define TS_END

#define A_TOP 131072u
#define A_EH (A_TOP + 1024u)

DEV cf csel(u32 g, cf a0, cf a1, cf a2, cf a3) {
  cf o;
  o.r = g == 0 ? a0.r : g == 1 ? a1.r : g == 2 ? a2.r : a3.r;
  o.i = g == 0 ? a0.i : g == 1 ? a1.i : g == 2 ? a2.i : a3.i;
  return o;
}

DEV void side_a(const Args& a, const u32 lane, const u32 w, const u32 chunk, const u32 s, const u32 k0) {
  TS_DECL
  const u32 g = lane >> 4, c = lane & 15;
  const u32 th = w >> 2, cg = w & 3;
  u32 p_lr, p_li, p_dt;
  u32x2 dsk[2];
  {
    const u32 o1 = (s * 64 + cg * 16 + c) * 2, o2 = s * 128 + th * 64 + g * 8;
    asm volatile("global_load_ushort %0, %1, %2" : "=v"(p_lr) : "v"(o1), "s"(a.in[1]) : "memory");
    asm volatile("global_load_ushort %0, %1, %2" : "=v"(p_li) : "v"(o1), "s"(a.in[2]) : "memory");
    asm volatile("global_load_ushort %0, %1, %2" : "=v"(p_dt) : "v"(o1), "s"(a.in[7]) : "memory");
    asm volatile("global_load_dwordx2 %0, %1, %2" : "=v"(dsk[0]) : "v"(o2), "s"(a.in[8]) : "memory");
    asm volatile("global_load_dwordx2 %0, %1, %2 offset:32" : "=v"(dsk[1]) : "v"(o2), "s"(a.in[8]) : "memory");
  }
  const rsrc_t rX = mkrsrc(a.in[0] + chunk * 65536u, 131072u);
  const rsrc_t rB = mkrsrc((w < 4 ? a.in[3] : a.in[4]) + s * 32768u, 65536u);
  const rsrc_t rCr = mkrsrc(a.in[5] + s * 32768u, 65536u);
  const rsrc_t rCi = mkrsrc(a.in[6] + s * 32768u, 65536u);
  const rsrc_t rW = mkrsrc(a.ws, WS_SIZE);
  u32 vx[2], vb[2];
#pragma unroll
  for (int i = 0; i < 2; i++) {
    const u32 rho = 8 * (2 * w + i) + (lane >> 3);
    const u32 unit = (lane & 7) ^ ((rho >> 1) & 7);
    const u32 tok = (rho & 64) + 16 * ((rho >> 2) & 3) + 4 * ((rho >> 4) & 3) + (rho & 3);
    vx[i] = tok * 1024 + unit * 16;
    vb[i] = (rho & 63) * 1024 + unit * 16;
  }
  const u32 vc = (8 * w + (lane >> 3)) * 1024 + (lane & 7) * 16;
  u32x4 cw[4][4];
  auto fetch = [&](int st, int i) {
    if (st < 8) {
      const u32 so = ((st + k0) & 7) * 128, d = (st & 3) * 32768 + w * 2048;
      if (i < 2) glds(vx[i], rX, so, d + i * 1024);
      else glds(vb[i - 2], rB, so, d + 16384 + (i - 2) * 1024);
    } else {
      cw[st - 8][i] = ld_pl((i & 1) ? rCi : rCr, vc, (st - 8) * 256 + (i >> 1) * 128);
    }
  };
#pragma unroll
  for (int st = 0; st < 3; st++)
#pragma unroll
    for (int i = 0; i < 4; i++) fetch(st, i);
  const u32 tag = *(const CST4 u32*)(a.ws + WS_EPA) + 1u;
  asm volatile("" ::"s"(tag));
  TS(2)
  asm volatile("s_waitcnt vmcnt(12)" : "+v"(p_lr), "+v"(p_li), "+v"(p_dt), "+v"(dsk[0]), "+v"(dsk[1])::"memory");
  cf lam, l2, lam16, lam64, c2, wg, wl[4];
  {
    const float L2E = 1.4426950408889634f;
    const float lr = bflo(p_lr), li = bflo(p_li);
    const float dt = bflo(cvtpk(__builtin_amdgcn_exp2f(bflo(p_dt) * L2E), 0.f));
    const float e = __builtin_amdgcn_exp2f(dt * lr * L2E);
    const float rev = __builtin_amdgcn_fractf(dt * li * 0.15915494309189535f);
    lam.r = e * __builtin_amdgcn_cosf(rev); lam.i = e * __builtin_amdgcn_sinf(rev);
    const float nr = lam.r - 1.f, ni = lam.i, rd = 2.f * __builtin_amdgcn_rcpf(lr * lr + li * li);
    c2.r = (nr * lr + ni * li) * rd; c2.i = (ni * lr - nr * li) * rd;
    l2 = cmul(lam, lam);
    const cf l4 = cmul(l2, l2), l8 = cmul(l4, l4);
    lam16 = cmul(l8, l8);
    const cf l32 = cmul(lam16, lam16), l48 = cmul(l32, lam16);
    lam64 = cmul(l32, l32);
    const cf mu = cmul(lam64, lam64), mu2 = cmul(mu, mu), mu3 = cmul(mu2, mu), mu4 = cmul(mu2, mu2);
    wg = csel(g, cf{1.f, 0.f}, lam16, l32, l48);
    wl[0] = csel(g, cf{1.f, 0.f}, mu, mu2, mu3);
    wl[1] = cmul(wl[0], mu4); wl[2] = cmul(wl[1], mu4); wl[3] = cmul(wl[2], mu4);
    pinc(lam); pinc(l2); pinc(lam16); pinc(lam64); pinc(c2); pinc(wg); pinc(wl[0]); pinc(wl[1]); pinc(wl[2]); pinc(wl[3]);
  }
  u32 vo[4], need = 0;
#pragma unroll
  for (int j = 0; j < 4; j++) {
    const int i = (int)chunk - 1 - (int)g - 4 * j;
    vo[j] = i >= 0 ? WS_GA + ((i * 8 + s) * 64 + cg * 16 + c) * 16 : OOB;
    need |= (i >= 0 ? 1u : 0u) << j;
  }
  u32 vga = WS_GA + ((chunk * 8 + s) * 64 + cg * 16 + c) * 16, aeh = A_EH + (cg * 16 + c) * 8;
  u32 vhs = WS_H + ((chunk * 8 + th * 4) * 32 + s * 4 + cg) * 1024 + g * 256 + c * 4;
  pin(vo[0], vo[1], vo[2], vo[3], need, vga, aeh, vhs);
  f32x4 ur[4], ui[4];
#pragma unroll
  for (int t = 0; t < 4; t++) { ur[t] = f32x4{0, 0, 0, 0}; ui[t] = f32x4{0, 0, 0, 0}; }
  const u32 ax0 = (th * 64 + c) * 128 + (g ^ (c >> 1)) * 16;
  const u32 ab0 = 16384 + (cg * 16 + c) * 128 + (g ^ (c >> 1)) * 16;
  const u32 st_skip = (s - k0) & 7;
  const u32 axs = (32 * cg + c) * 128 + ((4 * th + (g >> 1)) ^ (c >> 1)) * 16 + (g & 1) * 8;
  u32x2 xs[2][2];
#pragma unroll
  for (int i = 0; i < 2; i++) { xs[i][0] = u32x2{0, 0}; xs[i][1] = u32x2{0, 0}; }
  u32x4 f0[6], f1[6];
  auto rdf = [&](u32x4* f, int st, int kk) {
    const u32 sb = (st & 3) * 32768;
    f[4] = lds128((ab0 ^ (kk * 64)) + sb);
    f[0] = lds128((ax0 ^ (kk * 64)) + sb);
    f[5] = lds128((ab0 ^ (kk * 64)) + sb + 8192);
#pragma unroll
    for (int t = 1; t < 4; t++) f[t] = lds128((ax0 ^ (kk * 64)) + sb + t * 2048);
  };
  auto mm = [&](const u32x4* f, int nst, int i0) {
#pragma unroll
    for (int t = 0; t < 4; t++) {
      ur[t] = mfma(f[t], f[4], ur[t]);
      ui[t] = mfma(f[t], f[5], ui[t]);
      if (t == 0 || t == 2) { SB(); fetch(nst, i0 + (t >> 1)); SB(); }
    }
  };
#pragma unroll
  for (int st = 0; st < 8; st++) {
    SB();
    wait_bar<8>();
    if (st == 0) { TS(3) }
    if (st == 4) { TS(16) }
    if (st == 7) { TS(4) }
    rdf(f0, st, 0);
    SB();
    if (st > 0) mm(f1, st + 3, 0);
    else { fetch(3, 0); fetch(3, 1); }
    SB();
    rdf(f1, st, 1);
    if (st_skip == (u32)st) {
      const u32 sb = (st & 3) * 32768;
#pragma unroll
      for (int i = 0; i < 2; i++) { xs[i][0] = lds64(axs + sb + i * 2048); fence_c(); xs[i][1] = lds64((axs ^ 32) + sb + i * 2048); fence_c(); }
    }
    SB();
    mm(f0, st + 3, 2);
  }
  SB();
  mm(f1, 11, 0);
  SB();
  fetch(11, 2); fetch(11, 3);
  TS(5)
  cf e;
  {
    f32x2 er = lo2(ur[0]), ei = lo2(ui[0]);
#pragma unroll
    for (int k = 1; k < 8; k++) {
      const f32x2 R = (k & 1) ? hi2(ur[k >> 1]) : lo2(ur[k >> 1]), I = (k & 1) ? hi2(ui[k >> 1]) : lo2(ui[k >> 1]);
      const f32x2 nr = pfma(bc(-l2.i), ei, pfma(bc(l2.r), er, R));
      ei = pfma(bc(l2.i), er, pfma(bc(l2.r), ei, I));
      er = nr;
    }
    e = cfma(lam, cf{er.x, ei.x}, cf{er.y, ei.y});
  }
  cf eg[4];
  {
    auto r = __builtin_amdgcn_permlane32_swap(asu(e.r), asu(e.r), false, false);
    auto q = __builtin_amdgcn_permlane32_swap(asu(e.i), asu(e.i), false, false);
    auto r0 = __builtin_amdgcn_permlane16_swap(r[0], r[0], false, false);
    auto q0 = __builtin_amdgcn_permlane16_swap(q[0], q[0], false, false);
    auto r1 = __builtin_amdgcn_permlane16_swap(r[1], r[1], false, false);
    auto q1 = __builtin_amdgcn_permlane16_swap(q[1], q[1], false, false);
    eg[0].r = asf(r0[0]); eg[1].r = asf(r0[1]); eg[2].r = asf(r1[0]); eg[3].r = asf(r1[1]);
    eg[0].i = asf(q0[0]); eg[1].i = asf(q0[1]); eg[2].i = asf(q1[0]); eg[3].i = asf(q1[1]);
  }
  const cf s2 = cfma(lam16, eg[0], eg[1]), s3 = cfma(lam16, s2, eg[2]), eh = cfma(lam16, s3, eg[3]);
  if (th == 0 && g == 0) sts64(aeh, u32x2{asu(eh.r), asu(eh.i)});
  lds_bar();
  cf pre = csel(g, cf{0.f, 0.f}, eg[0], s2, s3);
  if (th == 1) {
    const u32x2 t = lds64(aeh);
    const cf e0{asf(t.x), asf(t.y)};
    const cf tot = cfma(lam64, e0, eh);
    if (g == 0) st_sc1(u32x4{asu(tot.r), tag, asu(tot.i), tag}, rW, vga);
    pre = cfma(wg, e0, pre);
    wg = cmul(wg, lam64);
  }
  TS(6)
  SB();
  f32x2 hr[8], hi[8], wr[8], wi[8];
  {
    const cf p2 = cmul(c2, pre);
    wg = cmul(c2, wg);
    cf h{p2.r, -p2.i};
    const cf lc{lam.r, -lam.i};
#pragma unroll
    for (int k = 0; k < 8; k++) {
      const f32x2 R = (k & 1) ? hi2(ur[k >> 1]) : lo2(ur[k >> 1]), I = (k & 1) ? hi2(ui[k >> 1]) : lo2(ui[k >> 1]);
      const f32x2 vr = pfma(bc(-c2.i), I, bc(c2.r) * R), vi = pfma(bc(-c2.i), R, bc(-c2.r) * I);
      const cf h0 = cfma(lc, h, cf{vr.x, vi.x});
      h = cfma(lc, h0, cf{vr.y, vi.y});
      hr[k] = f32x2{h0.r, h.r}; hi[k] = f32x2{h0.i, h.i};
    }
    wr[0] = f32x2{lam.r, l2.r}; wi[0] = f32x2{-lam.i, -l2.i};
#pragma unroll
    for (int k = 1; k < 8; k++) {
      wr[k] = pfma(bc(l2.i), wi[k - 1], bc(l2.r) * wr[k - 1]);
      wi[k] = pfma(bc(-l2.i), wr[k - 1], bc(l2.r) * wi[k - 1]);
    }
#pragma unroll
    for (int k = 0; k < 8; k++) pin(hr[k], hi[k], wr[k], wi[k]);
    pinc(wg);
  }
  SB();
  u32x4 gr[4];
#pragma unroll
  for (int j = 0; j < 4; j++) gr[j] = ld_sc1(rW, vo[j], 0);
  SB();
  TS(17)
  f32x4 acc[2][4];
#pragma unroll
  for (int i = 0; i < 2; i++) {
#pragma unroll
    for (int e2 = 0; e2 < 2; e2++) {
      const u32 p0 = cvtpk(bflo(xs[i][e2].x) * bflo(dsk[e2].x), bfhi(xs[i][e2].x) * bfhi(dsk[e2].x));
      const u32 p1 = cvtpk(bflo(xs[i][e2].y) * bflo(dsk[e2].y), bfhi(xs[i][e2].y) * bfhi(dsk[e2].y));
      acc[i][e2] = f32x4{bflo(p0), bfhi(p0), bflo(p1), bfhi(p1)};
      acc[i][2 + e2] = f32x4{0, 0, 0, 0};
      pin(acc[i][e2]);
    }
  }
  {
    const u32 u = lane & 7, pb = ((u >> 1) - 2 * s) & 15;
    const u32 rest = (w >> 1) * 1024 + (u & 1) * 512 + ((8 * (w & 1) + (lane >> 3)) ^ (2 * (pb & 3) + (u & 1))) * 16;
#pragma unroll
    for (int p = 0; p < 4; p++)
#pragma unroll
      for (int hf = 0; hf < 2; hf++) {
        const u32 ad = ((((pb + (p & 1) * 8 + hf * 4) & 15) << 12) | (rest ^ ((p >> 1) * 2048))) + (p >> 1) * 65536;
        const u32x4 R = cw[p][2 * hf], I = cw[p][2 * hf + 1];
        sts128(ad, u32x4{__builtin_amdgcn_perm(I.x, R.x, 0x05040100u), __builtin_amdgcn_perm(I.x, R.x, 0x07060302u),
                         __builtin_amdgcn_perm(I.y, R.y, 0x05040100u), __builtin_amdgcn_perm(I.y, R.y, 0x07060302u)});
        sts128(ad + 256, u32x4{__builtin_amdgcn_perm(I.z, R.z, 0x05040100u), __builtin_amdgcn_perm(I.z, R.z, 0x07060302u),
                               __builtin_amdgcn_perm(I.w, R.w, 0x05040100u), __builtin_amdgcn_perm(I.w, R.w, 0x07060302u)});
      }
  }
  SB();
  TS(18)
  {
#pragma unroll 1
    for (int it = 0; it < SPIN_CAP; it++) {
      u32 have = 0;
#pragma unroll
      for (int j = 0; j < 4; j++) have |= (((gr[j].y ^ tag) | (gr[j].w ^ tag)) == 0 ? 1u : 0u) << j;
      if (none((need & ~have) != 0)) break;
      fence_c();
#pragma unroll
      for (int j = 0; j < 4; j++) gr[j] = ld_sc1(rW, vo[j], 0);
    }
  }
  TS(7)
  {
    cf H = cmul(wl[0], cf{asf(gr[0].x), asf(gr[0].z)});
#pragma unroll
    for (int j = 1; j < 4; j++) H = cfma(wl[j], cf{asf(gr[j].x), asf(gr[j].z)}, H);
    H.r = red16(red32(H.r)); H.i = red16(red32(H.i));
    const cf z = cmul(wg, H);
#pragma unroll
    for (int k = 0; k < 8; k++) {
      const f32x2 yr = pfma(wi[k], bc(z.i), pfma(wr[k], bc(z.r), hr[k]));
      const f32x2 yi = pfma(wr[k], bc(-z.i), pfma(wi[k], bc(z.r), hi[k]));
      st32_sc1(cvtpk(yr.x, yi.x), rW, vhs + (k & 1) * 128, (k >> 1) * 32768);
      st32_sc1(cvtpk(yr.y, yi.y), rW, vhs + (k & 1) * 128 + 64, (k >> 1) * 32768);
    }
  }
  TS(8)
  wait_bar<0>();
  if (w == 0 && lane < 8) __builtin_amdgcn_raw_buffer_store_b64(u32x2{tag, tag}, rW, WS_FLA + (chunk * 8 + lane) * 128 + s * 8, 0, 16);
  TS(9)
  u32 cb[4];
#pragma unroll
  for (int i = 0; i < 4; i++) cb[i] = th * 65536 + g * 256 + ((c ^ (2 * i + (g >> 1))) * 16);
  u32x4 cfr[2][4];
  auto rdc = [&](int ii) {
#pragma unroll
    for (int ct = 0; ct < 4; ct++) cfr[ii & 1][ct] = lds128(cb[ii & 3] + ii * 4096 + ct * 1024);
  };
  rdc(0);
  {
    const u32 vf = WS_FLA + (chunk * 8 + s) * 128 + (th * 4 + (lane & 3)) * 8;
#pragma unroll 1
    for (int it = 0; it < SPIN_CAP; it++) {
      fence_c();
      const u32x2 f = ld64_sc1(rW, vf, 0);
      if (none(((f.x ^ tag) | (f.y ^ tag)) != 0)) break;
    }
  }
  TS(10)
  {
    u32x4 hf[16][2];
    const u32 vh = WS_H + ((chunk * 8 + 2 * cg) * 32 + th * 16) * 1024 + c * 64 + g * 16;
    auto ldh = [&](int ii, int i) { hf[ii][i] = ld_sc1(rW, vh, ((ii + 2 * s) & 15) * 1024 + i * 32768); };
#pragma unroll
    for (int ii = 0; ii < DEPA; ii++) { ldh(ii, 0); ldh(ii, 1); }
    SB();
    TS(11)
#pragma unroll
    for (int ii = 0; ii < 16; ii++) {
      if (ii < 15) rdc(ii + 1);
      SB();
#pragma unroll
      for (int ct = 0; ct < 4; ct++) {
        acc[0][ct] = mfma(cfr[ii & 1][ct], hf[ii][0], acc[0][ct]);
        acc[1][ct] = mfma(cfr[ii & 1][ct], hf[ii][1], acc[1][ct]);
        if ((ct == 0 || ct == 2) && ii + DEPA < 16) { SB(); ldh(ii + DEPA, ct >> 1); SB(); }
      }
      SB();
      if (ii == 0) { TS(12) }
    }
  }
  TS(13)
#pragma unroll
  for (int i = 0; i < 2; i++)
#pragma unroll
    for (int e2 = 0; e2 < 2; e2++) sts128(A_TOP + w * 4096 + (i * 2 + e2) * 1024 + lane * 16, __builtin_bit_cast(u32x4, acc[i][2 + e2]));
  lds_bar();
  TS(19)
  {
    const rsrc_t rY = mkrsrc(a.out, 8388608u);
    const u32 op = (chunk * 128 + (cg >> 1) * 64 + 16 * (c >> 2) + 8 * (cg & 1) + (c & 3)) * 4096 + (s * 64 + th * 32 + 4 * g) * 4;
#pragma unroll
    for (int i = 0; i < 2; i++)
#pragma unroll
      for (int e2 = 0; e2 < 2; e2++) {
        const f32x4 o = acc[i][e2] + __builtin_bit_cast(f32x4, lds128(A_TOP + (w ^ 4) * 4096 + (i * 2 + e2) * 1024 + lane * 16));
        __builtin_amdgcn_raw_buffer_store_b128(__builtin_bit_cast(u32x4, o), rY, op + i * 16384 + e2 * 64, 0, OAUX);
      }
  }
  if (chunk == 15 && s == 0 && w == 0 && lane == 0) __builtin_amdgcn_raw_buffer_store_b32(tag, rW, WS_EPA, 0, 16);
  TS(14)
  TS_END
}

#define B_SLOT 45056u
#define B_WO 90112u
#define B_PHI 155648u
#define B_KVI (B_PHI + 1024u)
#define B_ZI (B_PHI + 3072u)

DEV void softmax2(const f32x4* p, u32* ph) {
  float mx = __builtin_fmaxf(p[0][0], p[0][1]), mn = __builtin_fminf(p[0][0], p[0][1]);
#pragma unroll
  for (int i = 2; i < 16; i += 2) {
    mx = __builtin_fmaxf(__builtin_fmaxf(mx, p[i >> 2][i & 3]), p[i >> 2][(i & 3) + 1]);
    mn = __builtin_fminf(__builtin_fminf(mn, p[i >> 2][i & 3]), p[i >> 2][(i & 3) + 1]);
  }
  const float L = 1.4426950408889634f;
  const float a = -mx * L, b = mn * L;
  f32x2 e1[8], e2[8];
#pragma unroll
  for (int i = 0; i < 8; i++) {
    const f32x2 v = (i & 1) ? hi2(p[i >> 1]) : lo2(p[i >> 1]);
    const f32x2 x1 = pfma(v, bc(L), bc(a)), x2 = pfma(v, bc(-L), bc(b));
    e1[i] = f32x2{__builtin_amdgcn_exp2f(x1.x), __builtin_amdgcn_exp2f(x1.y)};
    e2[i] = f32x2{__builtin_amdgcn_exp2f(x2.x), __builtin_amdgcn_exp2f(x2.y)};
  }
  const f32x2 s1 = ((e1[0] + e1[1]) + (e1[2] + e1[3])) + ((e1[4] + e1[5]) + (e1[6] + e1[7]));
  const f32x2 s2 = ((e2[0] + e2[1]) + (e2[2] + e2[3])) + ((e2[4] + e2[5]) + (e2[6] + e2[7]));
  const float r1 = __builtin_amdgcn_rcpf(s1.x + s1.y), r2 = __builtin_amdgcn_rcpf(s2.x + s2.y);
#pragma unroll
  for (int i = 0; i < 8; i++) {
    const f32x2 y1 = e1[i] * bc(r1), y2 = e2[i] * bc(r2);
    ph[i] = cvtpk(y1.x, y1.y);
    ph[8 + i] = cvtpk(y2.x, y2.y);
  }
}
DEV u32x4 pack_gran(f32x4 v, u32 t_lo, u32 t_hi) {
  u32x4 o;
  o.x = ((asu(v[0]) + 0x200u) & 0xFFFFFC00u) | t_lo;
  o.y = ((asu(v[1]) + 0x200u) & 0xFFFFFC00u) | t_hi;
  o.z = ((asu(v[2]) + 0x200u) & 0xFFFFFC00u) | t_lo;
  o.w = ((asu(v[3]) + 0x200u) & 0xFFFFFC00u) | t_hi;
  return o;
}
DEV u32 gran_bad(u32x4 v, u32 t_lo, u32 t_hi) { return (v.x ^ t_lo) | (v.y ^ t_hi) | (v.z ^ t_lo) | (v.w ^ t_hi); }
DEV f32x4 gran_val(u32x4 v) { return f32x4{asf(v.x & 0xFFFFFC00u), asf(v.y & 0xFFFFFC00u), asf(v.z & 0xFFFFFC00u), asf(v.w & 0xFFFFFC00u)}; }

DEV void side_b(const Args& a, const u32 lane, const u32 w, const u32 chunk, const u32 pair, const u32 tt, const u32 jc, const u32 k0) {
  TS_DECL
  const u32 g = lane >> 4, c = lane & 15;
  const u32 tg = w & 3, hh = w >> 2;
  const u32 rq = c >> 2, q = c & 3;
  const rsrc_t rX = mkrsrc(a.in[9] + chunk * 131072u, 262144u);
  const rsrc_t rQ = mkrsrc(a.in[10], 1572864u);
  const rsrc_t rP = mkrsrc(a.in[11] + pair * 512u, 1024u);
  const rsrc_t rO = mkrsrc(a.in[12], 524288u);
  const rsrc_t rW = mkrsrc(a.ws, WS_SIZE);
  u32 vx[2], vw[2], vo[2];
#pragma unroll
  for (int i = 0; i < 2; i++) {
    vx[i] = (32 * w + 8 * i + (lane >> 3)) * 1024 + ((lane & 7) ^ ((4 * i + (lane >> 4)) & 7)) * 16;
    const u32 U = 48 * (2 * w + i) + lane, k = (U >> 2) & 63;
    vw[i] = k * 3072 + (U >> 8) * 1024 + pair * 64 + ((U & 3) ^ ((k >> 2) & 2)) * 16;
    vo[i] = (64 * w + 8 * i + (lane >> 3)) * 1024 + jc * 128 + ((lane & 7) ^ (((lane >> 3) & 2) | (i << 2))) * 16;
  }
  asm volatile("s_nop 4");
  if (w == 0) glds(lane * 16, rP, 0, B_PHI);
  auto fetch = [&](int st, int i) {
    if (st < 8) {
      const u32 kb = (st + k0) & 7, d = (st % 3) * B_SLOT;
      if (i < 4) glds(vx[i & 1], rX, kb * 128 + (i >> 1) * 16384, d + w * 4096 + i * 1024);
      else glds48(vw[i - 4], rQ, kb * 196608, d + 32768 + w * 1536 + (i - 4) * 768);
    } else {
      const int n = (st - 8) * 6 + i;
      if (n < 8) glds(vo[n & 1], rO, (n >> 1) * 16384, B_WO + w * 8192 + n * 1024);
    }
  };
#pragma unroll
  for (int st = 0; st < 2; st++)
#pragma unroll
    for (int i = 0; i < 6; i++) fetch(st, i);
  const u32 tag = *(const CST4 u32*)(a.ws + WS_EPB) + 1u;
  asm volatile("" ::"s"(tag));
  TS(2)
  f32x4 qa[4], ka[4], va[4];
#pragma unroll
  for (int t = 0; t < 4; t++) { qa[t] = f32x4{0, 0, 0, 0}; ka[t] = f32x4{0, 0, 0, 0}; va[t] = f32x4{0, 0, 0, 0}; }
  const u32 ax0 = (tg * 64 + c) * 128 + (g ^ (c >> 1)) * 16;
  const u32 aw0 = 32768 + (8 * g + rq) * 64 + ((2 * hh + (q >> 1)) ^ ((g & 1) << 1)) * 16 + (q & 1) * 8;
  u32x4 aphi[4][2];
  u32x4 f0[7], f1[7];
  auto rdf = [&](u32x4* f, int st, int kk) {
    const u32 sb = (st % 3) * B_SLOT, aw = aw0 + sb + kk * 2048;
    f[5] = cat(ldstr(aw + 4096), ldstr(aw + 4096 + 256));
#pragma unroll
    for (int t = 0; t < 4; t++) f[t] = lds128((ax0 ^ (kk * 64)) + sb + t * 2048);
    f[6] = cat(ldstr(aw + 8192), ldstr(aw + 8192 + 256));
    f[4] = cat(ldstr(aw), ldstr(aw + 256));
  };
  auto mm = [&](const u32x4* f, int nst, int i0) {
#pragma unroll
    for (int t = 0; t < 4; t++) { ka[t] = mfma(f[5], f[t], ka[t]); if (t == 1) { SB(); fetch(nst, i0); SB(); } }
#pragma unroll
    for (int t = 0; t < 4; t++) { va[t] = mfma(f[t], f[6], va[t]); if (t == 1) { SB(); fetch(nst, i0 + 1); SB(); } }
#pragma unroll
    for (int t = 0; t < 4; t++) { qa[t] = mfma(f[4], f[t], qa[t]); if (t == 1) { SB(); fetch(nst, i0 + 2); SB(); } }
  };
#pragma unroll
  for (int st = 0; st < 8; st++) {
    SB();
    wait_bar<6>();
    if (st == 0) { TS(3) }
    if (st == 4) { TS(16) }
    if (st == 7) { TS(4) }
    rdf(f0, st, 0);
    SB();
    if (st > 0) mm(f1, st + 2, 0);
    else { fetch(2, 0); fetch(2, 1); fetch(2, 2); }
    SB();
    rdf(f1, st, 1);
    if (st == 0) {
#pragma unroll
      for (int i = 0; i < 4; i++) {
        const u32x2 wd = ldstr(B_PHI + hh * 512 + (4 * g + rq) * 32 + i * 8);
#pragma unroll
        for (int h = 0; h < 2; h++) {
          aphi[i][h].x = rq == 2 * h ? wd.x : 0u; aphi[i][h].y = rq == 2 * h ? wd.y : 0u;
          aphi[i][h].z = rq == 2 * h + 1 ? wd.x : 0u; aphi[i][h].w = rq == 2 * h + 1 ? wd.y : 0u;
          pin(aphi[i][h]);
        }
      }
    }
    SB();
    mm(f0, st + 2, 3);
  }
  SB();
#pragma unroll
  for (int t = 0; t < 4; t++) ka[t] = mfma(f1[5], f1[t], ka[t]);
#pragma unroll
  for (int t = 0; t < 4; t++) va[t] = mfma(f1[t], f1[6], va[t]);
#pragma unroll
  for (int t = 0; t < 4; t++) qa[t] = mfma(f1[4], f1[t], qa[t]);
  SB();
  TS(5)
  const u32 t_lo = tag & 0x3FFu, t_hi = (tag >> 10) & 0x3FFu;
  const f32x4 z4 = f32x4{0, 0, 0, 0};
  {
    const u32x4 b01 = u32x4{cvtpk(ka[0][0], ka[0][1]), cvtpk(ka[0][2], ka[0][3]), cvtpk(ka[1][0], ka[1][1]), cvtpk(ka[1][2], ka[1][3])};
    const u32x4 b23 = u32x4{cvtpk(ka[2][0], ka[2][1]), cvtpk(ka[2][2], ka[2][3]), cvtpk(ka[3][0], ka[3][1]), cvtpk(ka[3][2], ka[3][3])};
    f32x4 p[4];
#pragma unroll
    for (int i = 0; i < 4; i++) p[i] = mfma(aphi[i][0], b01, z4);
#pragma unroll
    for (int i = 0; i < 4; i++) p[i] = mfma(aphi[i][1], b23, p[i]);
    const u32x4 v01 = u32x4{cvtpk(va[0][0], va[0][1]), cvtpk(va[0][2], va[0][3]), cvtpk(va[1][0], va[1][1]), cvtpk(va[1][2], va[1][3])};
    const u32x4 v23 = u32x4{cvtpk(va[2][0], va[2][1]), cvtpk(va[2][2], va[2][3]), cvtpk(va[3][0], va[3][1]), cvtpk(va[3][2], va[3][3])};
    u32 ph[16];
    softmax2(p, ph);
    const u32 wa = w * 4096 + lane * 64, sw = (lane >> 1) & 3;
#pragma unroll
    for (int u = 0; u < 4; u++) sts128(wa + ((u ^ sw) * 16), u32x4{ph[4 * u], ph[4 * u + 1], ph[4 * u + 2], ph[4 * u + 3]});
    const u32 ra = w * 4096 + (4 * g + rq) * 64 + (((q >> 1) ^ ((2 * g + (rq >> 1)) & 3)) * 16) + (q & 1) * 8;
    const u32x4 ones = u32x4{0x3F803F80u, 0x3F803F80u, 0x3F803F80u, 0x3F803F80u};
    f32x4 kv[2], zz[2];
#pragma unroll
    for (int F = 0; F < 2; F++) {
      const u32x4 a01 = cat(ldstr((ra ^ (F * 32))), ldstr((ra ^ (F * 32)) + 1024));
      const u32x4 a23 = cat(ldstr((ra ^ (F * 32)) + 2048), ldstr((ra ^ (F * 32)) + 3072));
      kv[F] = mfma(a01, v01, z4); zz[F] = mfma(a01, ones, z4);
      kv[F] = mfma(a23, v23, kv[F]); zz[F] = mfma(a23, ones, zz[F]);
    }
#pragma unroll
    for (int F = 0; F < 2; F++) {
      sts128(w * 4096 + F * 1024 + lane * 16, __builtin_bit_cast(u32x4, kv[F]));
      if (c == 0) sts128(w * 4096 + 2048 + (F * 4 + g) * 16, __builtin_bit_cast(u32x4, zz[F]));
    }
  }
  lds_bar();
  TS(6)
  if (w < 5) {
    const u32 ra = w < 4 ? (w >> 1) * 16384 + (w & 1) * 1024 + lane * 16 : ((lane >> 3) & 1) * 16384 + 2048 + (lane & 7) * 16;
    const f32x4 sum = (__builtin_bit_cast(f32x4, lds128(ra)) + __builtin_bit_cast(f32x4, lds128(ra + 4096))) +
                      (__builtin_bit_cast(f32x4, lds128(ra + 8192)) + __builtin_bit_cast(f32x4, lds128(ra + 12288)));
    const u32 vo2 = (w < 4 || lane < 16) ? WS_GB + (pair * 8 + chunk) * 4352 + (w * 64 + lane) * 16 : OOB;
    st_sc1(pack_gran(sum, t_lo, t_hi), rW, vo2);
  }
  TS(7)
  u32x4 bq[4];
  {
    const u32x4 b01 = u32x4{cvtpk(qa[0][0], qa[0][1]), cvtpk(qa[0][2], qa[0][3]), cvtpk(qa[1][0], qa[1][1]), cvtpk(qa[1][2], qa[1][3])};
    const u32x4 b23 = u32x4{cvtpk(qa[2][0], qa[2][1]), cvtpk(qa[2][2], qa[2][3]), cvtpk(qa[3][0], qa[3][1]), cvtpk(qa[3][2], qa[3][3])};
    f32x4 p[4];
#pragma unroll
    for (int i = 0; i < 4; i++) p[i] = mfma(aphi[i][0], b01, z4);
#pragma unroll
    for (int i = 0; i < 4; i++) p[i] = mfma(aphi[i][1], b23, p[i]);
    u32 ph[16];
    softmax2(p, ph);
#pragma unroll
    for (int d = 0; d < 4; d++) {
      auto s0 = __builtin_amdgcn_permlane32_swap(ph[d], ph[8 + d], false, false);
      auto s1 = __builtin_amdgcn_permlane32_swap(ph[4 + d], ph[12 + d], false, false);
      auto t0 = __builtin_amdgcn_permlane16_swap(s0[0], s1[0], false, false);
      auto t1 = __builtin_amdgcn_permlane16_swap(s0[1], s1[1], false, false);
      bq[0][d] = t0[0]; bq[1][d] = t0[1]; bq[2][d] = t1[0]; bq[3][d] = t1[1];
    }
    pin(bq[0], bq[1], bq[2], bq[3]);
  }
  TS(8)
  {
    const u32 vg = WS_GB + (pair * 8 + (lane >> 5) * 4) * 4352 + (w * 32 + (lane & 31)) * 16;
    const u32 vz = tg == 0 ? WS_GB + (pair * 8 + (lane >> 3)) * 4352 + (256 + hh * 8 + (lane & 7)) * 16 : OOB;
    u32x4 gr[4], gz;
    const u32 zmask = tg == 0 ? 0x3FFu : 0u;
#pragma unroll 1
    for (int it = 0; it < SPIN_CAP; it++) {
      fence_c();
#pragma unroll
      for (int j = 0; j < 4; j++) gr[j] = ld_sc1(rW, vg, j * 4352);
      gz = ld_sc1(rW, vz, 0);
      const u32 bad = (((gran_bad(gr[0], t_lo, t_hi) | gran_bad(gr[1], t_lo, t_hi)) | (gran_bad(gr[2], t_lo, t_hi) | gran_bad(gr[3], t_lo, t_hi))) & 0x3FFu) |
                      (gran_bad(gz, t_lo, t_hi) & zmask);
      if (none(bad != 0)) break;
    }
    TS(9)
    f32x4 sum = (gran_val(gr[0]) + gran_val(gr[1])) + (gran_val(gr[2]) + gran_val(gr[3]));
#pragma unroll
    for (int i = 0; i < 4; i++) sum[i] = red32(sum[i]);
    if (lane < 32) sts64(B_KVI + hh * 1024 + c * 64 + ((w >> 1) & 1) * 32 + (2 * (w & 1) + g) * 8, u32x2{cvtpk(sum[0], sum[1]), cvtpk(sum[2], sum[3])});
    if (tg == 0) {
      f32x4 z = gran_val(gz);
#pragma unroll
      for (int i = 0; i < 4; i++) { z[i] += asf(dpp<0x128>(asu(z[i]))); z[i] = red32(red16(z[i])); }
      if (lane < 8) sts64(B_ZI + hh * 64 + lane * 8, u32x2{cvtpk(z[0], z[1]), cvtpk(z[2], z[3])});
    }
  }
  lds_bar();
  {
    const u32x4 akv = lds128(B_KVI + hh * 1024 + c * 64 + g * 16);
    const u32x4 az = lds128(B_ZI + hh * 64 + g * 16);
    u32 X[4][2];
#pragma unroll
    for (int t = 0; t < 4; t++) {
      const f32x4 num = mfma(akv, bq[t], z4), den = mfma(az, bq[t], z4);
      const float r = __builtin_amdgcn_rcpf(den[0] + 1e-6f);
      X[t][0] = cvtpk(num[0] * r, num[1] * r); X[t][1] = cvtpk(num[2] * r, num[3] * r);
    }
    const u32 vs = WS_O + ((chunk * 16 + tg * 4 + (g & 1)) * 16 + pair) * 1024 + (2 * hh + (g >> 1)) * 256 + c * 16;
#pragma unroll
    for (int tp = 0; tp < 2; tp++) {
      auto s0 = __builtin_amdgcn_permlane16_swap(X[2 * tp][0], X[2 * tp + 1][0], false, false);
      auto s1 = __builtin_amdgcn_permlane16_swap(X[2 * tp][1], X[2 * tp + 1][1], false, false);
      st_sc1(u32x4{s0[0], s1[0], s0[1], s1[1]}, rW, vs + tp * 32768);
    }
  }
  TS(10)
  wait_bar<0>();
  if (w == 0 && lane < 16) __builtin_amdgcn_raw_buffer_store_b64(u32x2{tag, tag}, rW, WS_FLB + (chunk * 16 + lane) * 128 + pair * 8, 0, 16);
  TS(11)
  const u32 wb = B_WO + hh * 32768 + (8 * g + rq) * 128 + (((q >> 1) ^ ((rq & 2) | ((g & 1) << 2))) * 16) + (q & 1) * 8;
  u32x4 wfr[2][4];
  auto rdw = [&](int ii) {
    const u32 y = ((ii + jc) & 7) * 4096;
#pragma unroll
    for (int ct = 0; ct < 4; ct++) {
      const u32 ad = (wb ^ ((ct ^ (2 * hh)) * 32)) + y;
      wfr[ii & 1][ct] = cat(ldstr(ad), ldstr(ad + 512));
    }
  };
  rdw(0);
  {
    const u32 vf = WS_FLB + (tt * 8 + jc) * 128 + (hh * 8 + (lane & 7)) * 8;
#pragma unroll 1
    for (int it = 0; it < SPIN_CAP; it++) {
      fence_c();
      const u32x2 f = ld64_sc1(rW, vf, 0);
      if (none(((f.x ^ tag) | (f.y ^ tag)) != 0)) break;
    }
  }
  TS(12)
  f32x4 acc[2][4];
#pragma unroll
  for (int i = 0; i < 2; i++)
#pragma unroll
    for (int ct = 0; ct < 4; ct++) acc[i][ct] = z4;
  {
    u32x4 of[8][2];
    const u32 vh = WS_O + ((tt * 8 + 2 * tg) * 16 + hh * 8) * 1024 + lane * 16;
    auto ldo = [&](int ii, int i) { of[ii][i] = ld_sc1(rW, vh, ((ii + jc) & 7) * 1024 + i * 16384); };
#pragma unroll
    for (int ii = 0; ii < DEPB; ii++) { ldo(ii, 0); ldo(ii, 1); }
    SB();
#pragma unroll
    for (int ii = 0; ii < 8; ii++) {
      if (ii < 7) rdw(ii + 1);
      SB();
#pragma unroll
      for (int ct = 0; ct < 4; ct++) {
        acc[0][ct] = mfma(wfr[ii & 1][ct], of[ii][0], acc[0][ct]);
        acc[1][ct] = mfma(wfr[ii & 1][ct], of[ii][1], acc[1][ct]);
        if ((ct == 0 || ct == 2) && ii + DEPB < 8) { SB(); ldo(ii + DEPB, ct >> 1); SB(); }
      }
      SB();
      if (ii == 0) { TS(13) }
    }
  }
  TS(14)
#pragma unroll
  for (int i = 0; i < 2; i++)
#pragma unroll
    for (int e2 = 0; e2 < 2; e2++) sts128(w * 4096 + (i * 2 + e2) * 1024 + lane * 16, __builtin_bit_cast(u32x4, acc[i][2 + e2]));
  lds_bar();
  {
    const rsrc_t rY = mkrsrc(a.out, 8388608u);
    const u32 op = (tt * 128 + tg * 32 + c) * 4096 + (512 + jc * 64 + hh * 32 + 4 * g) * 4;
#pragma unroll
    for (int i = 0; i < 2; i++)
#pragma unroll
      for (int e2 = 0; e2 < 2; e2++) {
        const f32x4 o = acc[i][e2] + __builtin_bit_cast(f32x4, lds128((w ^ 4) * 4096 + (i * 2 + e2) * 1024 + lane * 16));
        const u32 p0 = cvtpk(o[0], o[1]), p1 = cvtpk(o[2], o[3]);
        __builtin_amdgcn_raw_buffer_store_b128(u32x4{p0 << 16, p0 & 0xffff0000u, p1 << 16, p1 & 0xffff0000u}, rY, op + i * 65536 + e2 * 64, 0, OAUX);
      }
  }
  if (tt == 0 && jc == 0 && w == 0 && lane == 0) __builtin_amdgcn_raw_buffer_store_b32(tag, rW, WS_EPB, 0, 16);
  TS(15)
  TS_END
}

__global__ __launch_bounds__(512, 2) void fused_s5_hedgehog(const Args a) {
  __shared__ __attribute__((aligned(1024))) char smem[163840];
  asm volatile("" ::"s"((u32)(size_t)(LDS3 char*)smem) : "memory");
  const u32 b = blockIdx.x, xcd = b & 7, j = b >> 3;
  const u32 lane = threadIdx.x & 63, w = rfl(threadIdx.x >> 6);
  if (xcd < 4) {
    const u32 k0 = ROTA == 0 ? 0u : ROTA == 1 ? 4u * ((j ^ (j >> 3)) & 1) : ((j + 2 * (j >> 3)) & 7);
    side_a(a, lane, w, xcd * 4 + (j >> 3), j & 7, k0);
  } else {
    const u32 x = xcd - 4;
    const u32 k0 = ROTB == 0 ? 0u : ROTB == 1 ? 4u * ((j ^ (j >> 3)) & 1) : ((j + 2 * (j >> 3)) & 7);
    side_b(a, lane, w, (x & 1) * 4 + (j >> 3), (x >> 1) * 8 + (j & 7), x * 4 + (j >> 3), j & 7, k0);
  }
}

extern "C" void kernel_launch(void* const* d_in, const int* in_sizes, int n_in, void* d_out, int out_size, void* d_ws,
                              size_t ws_size, hipStream_t stream) {
  Args a;
  for (int i = 0; i < 13; i++) a.in[i] = (const u16*)d_in[i];
  a.out = (float*)d_out;
  a.ws = (char*)d_ws;
  fused_s5_hedgehog<<<256, 512, 0, stream>>>(a);
}
```

```cpp
#define TSTAMP
#define K1 8
#define I1 0
#define K2 9
#define I2 0
#include <hip/hip_runtime.h>
#pragma clang diagnostic ignored "-Winline-asm"

typedef unsigned short u16;
typedef unsigned int u32;
typedef unsigned long long u64;
typedef float f32x4 __attribute__((ext_vector_type(4)));
typedef u32 u32x4 __attribute__((ext_vector_type(4)));
typedef u32 u32x2 __attribute__((ext_vector_type(2)));
typedef __bf16 bf16x8 __attribute__((ext_vector_type(8)));
typedef short s16x4 __attribute__((ext_vector_type(4)));
typedef __amdgpu_buffer_rsrc_t rsrc_t;
#define LDS3 __attribute__((address_space(3)))
#define CST4 __attribute__((address_space(4)))
#define DEV __device__ __forceinline__

#define ROTA 1
#define ROTB 1
#define OAUX 16
#define DEPA 5
#define DEPB 5

#define WS_EPA 0u
#define WS_EPB 256u
#define WS_FLA 4096u
#define WS_FLB 20480u
#define WS_GA 65536u
#define WS_GB 196608u
#define WS_H 1048576u
#define WS_O 5242880u
#define WS_SIZE 7340032u
#define WS_TS 8388608u
#define OOB 0xFFFFFF00u
#define SPIN_CAP (1 << 20)

struct Args {
  const u16* in[13];
  float* out;
  char* ws;
};

DEV u32 rfl(u32 x) { return __builtin_amdgcn_readfirstlane(x); }
DEV rsrc_t mkrsrc(const void* p, u32 bytes) { return __builtin_amdgcn_make_buffer_rsrc((void*)p, (short)0, (int)bytes, 0x00020000); }
DEV void glds(u32 voff, rsrc_t srd, u32 soff, u32 ldst) {
  asm volatile("s_mov_b32 m0, %0\n\ts_nop 0\n\tbuffer_load_dwordx4 %1, %2, %3 offen lds" ::"s"(ldst), "v"(voff), "s"(srd), "s"(soff) : "m0", "memory");
}
DEV void glds48(u32 voff, rsrc_t srd, u32 soff, u32 ldst) {
  asm volatile("s_mov_b32 m0, %0\n\ts_mov_b32 exec_hi, 0xffff\n\tbuffer_load_dwordx4 %1, %2, %3 offen lds\n\ts_mov_b32 exec_hi, -1" ::"s"(ldst), "v"(voff), "s"(srd), "s"(soff) : "m0", "memory");
}
template <int N> DEV void wait_bar() { asm volatile("s_waitcnt vmcnt(%0) lgkmcnt(0)\n\ts_barrier" ::"n"(N) : "memory"); }
DEV void lds_bar() { asm volatile("s_waitcnt lgkmcnt(0)\n\ts_barrier" ::: "memory"); }
DEV void raw_bar() { asm volatile("s_barrier" ::: "memory"); }
DEV void fence_c() { asm volatile("" ::: "memory"); }
DEV u32x4 lds128(u32 a) { return *reinterpret_cast<const LDS3 u32x4*>(a); }
DEV u32x2 lds64(u32 a) { return *reinterpret_cast<const LDS3 u32x2*>(a); }
DEV u32 lds16(u32 a) { return *reinterpret_cast<const LDS3 u16*>(a); }
DEV void sts128(u32 a, u32x4 v) { *reinterpret_cast<LDS3 u32x4*>(a) = v; }
DEV void sts64(u32 a, u32x2 v) { *reinterpret_cast<LDS3 u32x2*>(a) = v; }
DEV u32x2 ldstr(u32 a) { return __builtin_bit_cast(u32x2, __builtin_amdgcn_ds_read_tr16_b64_v4i16(reinterpret_cast<LDS3 s16x4*>(a))); }
DEV f32x4 mfma(u32x4 a, u32x4 b, f32x4 c) {
  return __builtin_amdgcn_mfma_f32_16x16x32_bf16(__builtin_bit_cast(bf16x8, a), __builtin_bit_cast(bf16x8, b), c, 0, 0, 0);
}
typedef float f32x2 __attribute__((ext_vector_type(2)));
typedef __bf16 bf16x2 __attribute__((ext_vector_type(2)));
DEV u32 cvtpk(float lo, float hi) { return __builtin_bit_cast(u32, __builtin_convertvector(f32x2{lo, hi}, bf16x2)); }
DEV float asf(u32 x) { return __uint_as_float(x); }
DEV u32 asu(float x) { return __float_as_uint(x); }
DEV float bflo(u32 x) { return asf(x << 16); }
DEV float bfhi(u32 x) { return asf(x & 0xffff0000u); }
DEV u32x4 cat(u32x2 a, u32x2 b) { u32x4 r; r.x = a.x; r.y = a.y; r.z = b.x; r.w = b.y; return r; }
DEV u32x4 ld_sc1(rsrc_t r, u32 voff, u32 soff) { return __builtin_amdgcn_raw_buffer_load_b128(r, voff, soff, 16); }
DEV u32x2 ld64_sc1(rsrc_t r, u32 voff, u32 soff) { return __builtin_amdgcn_raw_buffer_load_b64(r, voff, soff, 16); }
DEV u32x4 ld_pl(rsrc_t r, u32 voff, u32 soff) { return __builtin_amdgcn_raw_buffer_load_b128(r, voff, soff, 0); }
DEV void st_sc1(u32x4 v, rsrc_t r, u32 voff) { __builtin_amdgcn_raw_buffer_store_b128(v, r, voff, 0, 16); }
DEV void st32_sc1(u32 v, rsrc_t r, u32 voff, u32 soff) { __builtin_amdgcn_raw_buffer_store_b32(v, r, voff, soff, 16); }
#define SB() __builtin_amdgcn_sched_barrier(0)
template <class T> DEV void pin(T& x) { asm volatile("" : "+v"(x)); }
template <class T, class... R> DEV void pin(T& x, R&... r) { pin(x); pin(r...); }
DEV f32x2 pfma(f32x2 a, f32x2 b, f32x2 c) { return __builtin_elementwise_fma(a, b, c); }
DEV f32x2 bc(float x) { return f32x2{x, x}; }
DEV f32x2 lo2(f32x4 v) { return __builtin_shufflevector(v, v, 0, 1); }
DEV f32x2 hi2(f32x4 v) { return __builtin_shufflevector(v, v, 2, 3); }
DEV bool none(bool bad) { return __builtin_amdgcn_ballot_w64(bad) == 0; }
DEV float red32(float x) { auto r = __builtin_amdgcn_permlane32_swap(asu(x), asu(x), false, false); return asf(r[0]) + asf(r[1]); }
DEV float red16(float x) { auto r = __builtin_amdgcn_permlane16_swap(asu(x), asu(x), false, false); return asf(r[0]) + asf(r[1]); }
template <int CTRL> DEV u32 dpp(u32 x) { return __builtin_amdgcn_update_dpp(0u, x, CTRL, 0xf, 0xf, true); }
struct cf { float r, i; };
DEV void pinc(cf& x) { asm volatile("" : "+v"(x.r), "+v"(x.i)); }
DEV cf cmul(cf a, cf b) { cf o; o.r = a.r * b.r - a.i * b.i; o.i = a.r * b.i + a.i * b.r; return o; }
DEV cf cfma(cf a, cf b, cf c) {
  cf o; o.r = __builtin_fmaf(a.r, b.r, c.r); o.i = __builtin_fmaf(a.r, b.i, c.i);
  o.r = __builtin_fmaf(-a.i, b.i, o.r); o.i = __builtin_fmaf(a.i, b.r, o.i); return o;
}

#define NTS 24
#define TS_DECL u64 ts_[NTS]; _Pragma("unroll") for (int i_ = 0; i_ < NTS; i_++) ts_[i_] = 0; ts_[0] = rt_(); ts_[1] = ct_();
#define TS(i) ts_[i] = ct_();
#define TS_END                                                                                   \
  ts_[NTS - 1] = rt_();                                                                          \
  if (lane == 0) {                                                                               \
    u64* p_ = (u64*)(a.ws + WS_TS) + (size_t)(blockIdx.x * 8 + w) * NTS;                         \
    _Pragma("unroll") for (int i_ = 0; i_ < NTS; i_++) p_[i_] = ts_[i_];                         \
  }
DEV u64 rt_() { u64 t; asm volatile("s_memrealtime %0\n\ts_waitcnt lgkmcnt(0)" : "=s"(t)::"memory"); return t; }
DEV u64 ct_() { u64 t; asm volatile("s_memtime %0\n\ts_waitcnt lgkmcnt(0)" : "=s"(t)::"memory"); return t; }

#define A_TOP 131072u
#define A_EH (A_TOP + 1024u)

DEV cf csel(u32 g, cf a0, cf a1, cf a2, cf a3) {
  cf o;
  o.r = g == 0 ? a0.r : g == 1 ? a1.r : g == 2 ? a2.r : a3.r;
  o.i = g == 0 ? a0.i : g == 1 ? a1.i : g == 2 ? a2.i : a3.i;
  return o;
}

DEV void side_a(const Args& a, const u32 lane, const u32 w, const u32 chunk, const u32 s, const u32 k0) {
  TS_DECL
  const u32 g = lane >> 4, c = lane & 15;
  const u32 th = w >> 2, cg = w & 3;
  u32 p_lr, p_li, p_dt;
  u32x2 dsk[2];
  {
    const u32 o1 = (s * 64 + cg * 16 + c) * 2, o2 = s * 128 + th * 64 + g * 8;
    asm volatile("global_load_ushort %0, %1, %2" : "=v"(p_lr) : "v"(o1), "s"(a.in[1]) : "memory");
    asm volatile("global_load_ushort %0, %1, %2" : "=v"(p_li) : "v"(o1), "s"(a.in[2]) : "memory");
    asm volatile("global_load_ushort %0, %1, %2" : "=v"(p_dt) : "v"(o1), "s"(a.in[7]) : "memory");
    asm volatile("global_load_dwordx2 %0, %1, %2" : "=v"(dsk[0]) : "v"(o2), "s"(a.in[8]) : "memory");
    asm volatile("global_load_dwordx2 %0, %1, %2 offset:32" : "=v"(dsk[1]) : "v"(o2), "s"(a.in[8]) : "memory");
  }
  const rsrc_t rX = mkrsrc(a.in[0] + chunk * 65536u, 131072u);
  const rsrc_t rB = mkrsrc((w < 4 ? a.in[3] : a.in[4]) + s * 32768u, 65536u);
  const rsrc_t rCr = mkrsrc(a.in[5] + s * 32768u, 65536u);
  const rsrc_t rCi = mkrsrc(a.in[6] + s * 32768u, 65536u);
  const rsrc_t rW = mkrsrc(a.ws, WS_SIZE);
  u32 vx[2], vb[2];
#pragma unroll
  for (int i = 0; i < 2; i++) {
    const u32 rho = 8 * (2 * w + i) + (lane >> 3);
    const u32 unit = (lane & 7) ^ ((rho >> 1) & 7);
    const u32 tok = (rho & 64) + 16 * ((rho >> 2) & 3) + 4 * ((rho >> 4) & 3) + (rho & 3);
    vx[i] = tok * 1024 + unit * 16;
    vb[i] = (rho & 63) * 1024 + unit * 16;
  }
  const u32 vc = (8 * w + (lane >> 3)) * 1024 + (lane & 7) * 16;
  u32x4 cw[4][4];
  auto fetch = [&](int st, int i) {
    if (st < 8) {
      const u32 so = ((st + k0) & 7) * 128, d = (st & 3) * 32768 + w * 2048;
      if (i < 2) glds(vx[i], rX, so, d + i * 1024);
      else glds(vb[i - 2], rB, so, d + 16384 + (i - 2) * 1024);
    } else {
      cw[st - 8][i] = ld_pl((i & 1) ? rCi : rCr, vc, (st - 8) * 256 + (i >> 1) * 128);
    }
  };
#pragma unroll
  for (int st = 0; st < 3; st++)
#pragma unroll
    for (int i = 0; i < 4; i++) fetch(st, i);
  const u32 tag = *(const CST4 u32*)(a.ws + WS_EPA) + 1u;
  asm volatile("" ::"s"(tag));
  TS(2)
  asm volatile("s_waitcnt vmcnt(12)" : "+v"(p_lr), "+v"(p_li), "+v"(p_dt), "+v"(dsk[0]), "+v"(dsk[1])::"memory");
  cf lam, l2, lam16, lam64, c2, wg, wl[4];
  {
    const float L2E = 1.4426950408889634f;
    const float lr = bflo(p_lr), li = bflo(p_li);
    const float dt = bflo(cvtpk(__builtin_amdgcn_exp2f(bflo(p_dt) * L2E), 0.f));
    const float e = __builtin_amdgcn_exp2f(dt * lr * L2E);
    const float rev = __builtin_amdgcn_fractf(dt * li * 0.15915494309189535f);
    lam.r = e * __builtin_amdgcn_cosf(rev); lam.i = e * __builtin_amdgcn_sinf(rev);
    const float nr = lam.r - 1.f, ni = lam.i, rd = 2.f * __builtin_amdgcn_rcpf(lr * lr + li * li);
    c2.r = (nr * lr + ni * li) * rd; c2.i = (ni * lr - nr * li) * rd;
    l2 = cmul(lam, lam);
    const cf l4 = cmul(l2, l2), l8 = cmul(l4, l4);
    lam16 = cmul(l8, l8);
    const cf l32 = cmul(lam16, lam16), l48 = cmul(l32, lam16);
    lam64 = cmul(l32, l32);
    const cf mu = cmul(lam64, lam64), mu2 = cmul(mu, mu), mu3 = cmul(mu2, mu), mu4 = cmul(mu2, mu2);
    wg = csel(g, cf{1.f, 0.f}, lam16, l32, l48);
    wl[0] = csel(g, cf{1.f, 0.f}, mu, mu2, mu3);
    wl[1] = cmul(wl[0], mu4); wl[2] = cmul(wl[1], mu4); wl[3] = cmul(wl[2], mu4);
    pinc(lam); pinc(l2); pinc(lam16); pinc(lam64); pinc(c2); pinc(wg); pinc(wl[0]); pinc(wl[1]); pinc(wl[2]); pinc(wl[3]);
  }
  u32 vo[4], need = 0;
#pragma unroll
  for (int j = 0; j < 4; j++) {
    const int i = (int)chunk - 1 - (int)g - 4 * j;
    vo[j] = i >= 0 ? WS_GA + ((i * 8 + s) * 64 + cg * 16 + c) * 16 : OOB;
    need |= (i >= 0 ? 1u : 0u) << j;
  }
  u32 vga = WS_GA + ((chunk * 8 + s) * 64 + cg * 16 + c) * 16, aeh = A_EH + (cg * 16 + c) * 8;
  u32 vhs = WS_H + ((chunk * 8 + th * 4) * 32 + s * 4 + cg) * 1024 + g * 256 + c * 4;
  pin(vo[0], vo[1], vo[2], vo[3], need, vga, aeh, vhs);
  f32x4 ur[4], ui[4];
#pragma unroll
  for (int t = 0; t < 4; t++) { ur[t] = f32x4{0, 0, 0, 0}; ui[t] = f32x4{0, 0, 0, 0}; }
  const u32 ax0 = (th * 64 + c) * 128 + (g ^ (c >> 1)) * 16;
  const u32 ab0 = 16384 + (cg * 16 + c) * 128 + (g ^ (c >> 1)) * 16;
  const u32 st_skip = (s - k0) & 7;
  const u32 axs = (32 * cg + c) * 128 + ((4 * th + (g >> 1)) ^ (c >> 1)) * 16 + (g & 1) * 8;
  u32x2 xs[2][2];
#pragma unroll
  for (int i = 0; i < 2; i++) { xs[i][0] = u32x2{0, 0}; xs[i][1] = u32x2{0, 0}; }
  u32x4 f0[6], f1[6];
  auto rdf = [&](u32x4* f, int st, int kk) {
    const u32 sb = (st & 3) * 32768;
    f[4] = lds128((ab0 ^ (kk * 64)) + sb);
    f[0] = lds128((ax0 ^ (kk * 64)) + sb);
    f[5] = lds128((ab0 ^ (kk * 64)) + sb + 8192);
#pragma unroll
    for (int t = 1; t < 4; t++) f[t] = lds128((ax0 ^ (kk * 64)) + sb + t * 2048);
  };
  auto mm = [&](const u32x4* f, int nst, int i0) {
#pragma unroll
    for (int t = 0; t < 4; t++) {
      ur[t] = mfma(f[t], f[4], ur[t]);
      ui[t] = mfma(f[t], f[5], ui[t]);
      if (t == 0 || t == 2) { SB(); fetch(nst, i0 + (t >> 1)); SB(); }
    }
  };
#pragma unroll
  for (int st = 0; st < 8; st++) {
    SB();
    wait_bar<8>();
    if (st == 0) { TS(3) }
    if (st == 4) { TS(16) }
    if (st == 7) { TS(4) }
    rdf(f0, st, 0);
    SB();
    if (st > 0) mm(f1, st + 3, 0);
    else { fetch(3, 0); fetch(3, 1); }
    SB();
    rdf(f1, st, 1);
    if (st_skip == (u32)st) {
      const u32 sb = (st & 3) * 32768;
#pragma unroll
      for (int i = 0; i < 2; i++) { xs[i][0] = lds64(axs + sb + i * 2048); fence_c(); xs[i][1] = lds64((axs ^ 32) + sb + i * 2048); fence_c(); }
    }
    SB();
    mm(f0, st + 3, 2);
  }
  SB();
  mm(f1, 11, 0);
  SB();
  fetch(11, 2); fetch(11, 3);
  TS(5)
  cf e;
  {
    f32x2 er = lo2(ur[0]), ei = lo2(ui[0]);
#pragma unroll
    for (int k = 1; k < 8; k++) {
      const f32x2 R = (k & 1) ? hi2(ur[k >> 1]) : lo2(ur[k >> 1]), I = (k & 1) ? hi2(ui[k >> 1]) : lo2(ui[k >> 1]);
      const f32x2 nr = pfma(bc(-l2.i), ei, pfma(bc(l2.r), er, R));
      ei = pfma(bc(l2.i), er, pfma(bc(l2.r), ei, I));
      er = nr;
    }
    e = cfma(lam, cf{er.x, ei.x}, cf{er.y, ei.y});
  }
  cf eg[4];
  {
    auto r = __builtin_amdgcn_permlane32_swap(asu(e.r), asu(e.r), false, false);
    auto q = __builtin_amdgcn_permlane32_swap(asu(e.i), asu(e.i), false, false);
    auto r0 = __builtin_amdgcn_permlane16_swap(r[0], r[0], false, false);
    auto q0 = __builtin_amdgcn_permlane16_swap(q[0], q[0], false, false);
    auto r1 = __builtin_amdgcn_permlane16_swap(r[1], r[1], false, false);
    auto q1 = __builtin_amdgcn_permlane16_swap(q[1], q[1], false, false);
    eg[0].r = asf(r0[0]); eg[1].r = asf(r0[1]); eg[2].r = asf(r1[0]); eg[3].r = asf(r1[1]);
    eg[0].i = asf(q0[0]); eg[1].i = asf(q0[1]); eg[2].i = asf(q1[0]); eg[3].i = asf(q1[1]);
  }
  const cf s2 = cfma(lam16, eg[0], eg[1]), s3 = cfma(lam16, s2, eg[2]), eh = cfma(lam16, s3, eg[3]);
  if (th == 0 && g == 0) sts64(aeh, u32x2{asu(eh.r), asu(eh.i)});
  lds_bar();
  cf pre = csel(g, cf{0.f, 0.f}, eg[0], s2, s3);
  if (th == 1) {
    const u32x2 t = lds64(aeh);
    const cf e0{asf(t.x), asf(t.y)};
    const cf tot = cfma(lam64, e0, eh);
    if (g == 0) st_sc1(u32x4{asu(tot.r), tag, asu(tot.i), tag}, rW, vga);
    pre = cfma(wg, e0, pre);
    wg = cmul(wg, lam64);
  }
  TS(6)
  SB();
  f32x2 hr[8], hi[8], wr[8], wi[8];
  {
    const cf p2 = cmul(c2, pre);
    wg = cmul(c2, wg);
    cf h{p2.r, -p2.i};
    const cf lc{lam.r, -lam.i};
#pragma unroll
    for (int k = 0; k < 8; k++) {
      const f32x2 R = (k & 1) ? hi2(ur[k >> 1]) : lo2(ur[k >> 1]), I = (k & 1) ? hi2(ui[k >> 1]) : lo2(ui[k >> 1]);
      const f32x2 vr = pfma(bc(-c2.i), I, bc(c2.r) * R), vi = pfma(bc(-c2.i), R, bc(-c2.r) * I);
      const cf h0 = cfma(lc, h, cf{vr.x, vi.x});
      h = cfma(lc, h0, cf{vr.y, vi.y});
      hr[k] = f32x2{h0.r, h.r}; hi[k] = f32x2{h0.i, h.i};
    }
    wr[0] = f32x2{lam.r, l2.r}; wi[0] = f32x2{-lam.i, -l2.i};
#pragma unroll
    for (int k = 1; k < 8; k++) {
      wr[k] = pfma(bc(l2.i), wi[k - 1], bc(l2.r) * wr[k - 1]);
      wi[k] = pfma(bc(-l2.i), wr[k - 1], bc(l2.r) * wi[k - 1]);
    }
#pragma unroll
    for (int k = 0; k < 8; k++) pin(hr[k], hi[k], wr[k], wi[k]);
    pinc(wg);
  }
  SB();
  u32x4 gr[4];
#pragma unroll
  for (int j = 0; j < 4; j++) gr[j] = ld_sc1(rW, vo[j], 0);
  SB();
  TS(17)
  f32x4 acc[2][4];
#pragma unroll
  for (int i = 0; i < 2; i++) {
#pragma unroll
    for (int e2 = 0; e2 < 2; e2++) {
      const u32 p0 = cvtpk(bflo(xs[i][e2].x) * bflo(dsk[e2].x), bfhi(xs[i][e2].x) * bfhi(dsk[e2].x));
      const u32 p1 = cvtpk(bflo(xs[i][e2].y) * bflo(dsk[e2].y), bfhi(xs[i][e2].y) * bfhi(dsk[e2].y));
      acc[i][e2] = f32x4{bflo(p0), bfhi(p0), bflo(p1), bfhi(p1)};
      acc[i][2 + e2] = f32x4{0, 0, 0, 0};
      pin(acc[i][e2]);
    }
  }
  {
    const u32 u = lane & 7, pb = ((u >> 1) - 2 * s) & 15;
    const u32 rest = (w >> 1) * 1024 + (u & 1) * 512 + ((8 * (w & 1) + (lane >> 3)) ^ (2 * (pb & 3) + (u & 1))) * 16;
#pragma unroll
    for (int p = 0; p < 4; p++)
#pragma unroll
      for (int hf = 0; hf < 2; hf++) {
        const u32 ad = ((((pb + (p & 1) * 8 + hf * 4) & 15) << 12) | (rest ^ ((p >> 1) * 2048))) + (p >> 1) * 65536;
        const u32x4 R = cw[p][2 * hf], I = cw[p][2 * hf + 1];
        sts128(ad, u32x4{__builtin_amdgcn_perm(I.x, R.x, 0x05040100u), __builtin_amdgcn_perm(I.x, R.x, 0x07060302u),
                         __builtin_amdgcn_perm(I.y, R.y, 0x05040100u), __builtin_amdgcn_perm(I.y, R.y, 0x07060302u)});
        sts128(ad + 256, u32x4{__builtin_amdgcn_perm(I.z, R.z, 0x05040100u), __builtin_amdgcn_perm(I.z, R.z, 0x07060302u),
                               __builtin_amdgcn_perm(I.w, R.w, 0x05040100u), __builtin_amdgcn_perm(I.w, R.w, 0x07060302u)});
      }
  }
  SB();
  TS(18)
  {
#pragma unroll 1
    for (int it = 0; it < SPIN_CAP; it++) {
      u32 have = 0;
#pragma unroll
      for (int j = 0; j < 4; j++) have |= (((gr[j].y ^ tag) | (gr[j].w ^ tag)) == 0 ? 1u : 0u) << j;
      if (none((need & ~have) != 0)) break;
      fence_c();
#pragma unroll
      for (int j = 0; j < 4; j++) gr[j] = ld_sc1(rW, vo[j], 0);
    }
  }
  TS(7)
  {
    cf H = cmul(wl[0], cf{asf(gr[0].x), asf(gr[0].z)});
#pragma unroll
    for (int j = 1; j < 4; j++) H = cfma(wl[j], cf{asf(gr[j].x), asf(gr[j].z)}, H);
    H.r = red16(red32(H.r)); H.i = red16(red32(H.i));
    const cf z = cmul(wg, H);
#pragma unroll
    for (int k = 0; k < 8; k++) {
      const f32x2 yr = pfma(wi[k], bc(z.i), pfma(wr[k], bc(z.r), hr[k]));
      const f32x2 yi = pfma(wr[k], bc(-z.i), pfma(wi[k], bc(z.r), hi[k]));
      st32_sc1(cvtpk(yr.x, yi.x), rW, vhs + (k & 1) * 128, (k >> 1) * 32768);
      st32_sc1(cvtpk(yr.y, yi.y), rW, vhs + (k & 1) * 128 + 64, (k >> 1) * 32768);
    }
  }
  TS(8)
  wait_bar<0>();
  if (w == 0 && lane < 8) __builtin_amdgcn_raw_buffer_store_b64(u32x2{tag, tag}, rW, WS_FLA + (chunk * 8 + lane) * 128 + s * 8, 0, 16);
  TS(9)
  u32 cb[4];
#pragma unroll
  for (int i = 0; i < 4; i++) cb[i] = th * 65536 + g * 256 + ((c ^ (2 * i + (g >> 1))) * 16);
  u32x4 cfr[2][4];
  auto rdc = [&](int ii) {
#pragma unroll
    for (int ct = 0; ct < 4; ct++) cfr[ii & 1][ct] = lds128(cb[ii & 3] + ii * 4096 + ct * 1024);
  };
  rdc(0);
  {
    const u32 vf = WS_FLA + (chunk * 8 + s) * 128 + (th * 4 + (lane & 3)) * 8;
#pragma unroll 1
    for (int it = 0; it < SPIN_CAP; it++) {
      fence_c();
      const u32x2 f = ld64_sc1(rW, vf, 0);
      if (none(((f.x ^ tag) | (f.y ^ tag)) != 0)) break;
    }
  }
  TS(10)
  {
    u32x4 hf[16][2];
    const u32 vh = WS_H + ((chunk * 8 + 2 * cg) * 32 + th * 16) * 1024 + c * 64 + g * 16;
    auto ldh = [&](int ii, int i) { hf[ii][i] = ld_sc1(rW, vh, ((ii + 2 * s) & 15) * 1024 + i * 32768); };
#pragma unroll
    for (int ii = 0; ii < DEPA; ii++) { ldh(ii, 0); ldh(ii, 1); }
    SB();
    TS(11)
#pragma unroll
    for (int ii = 0; ii < 16; ii++) {
      if (ii < 15) rdc(ii + 1);
      SB();
#pragma unroll
      for (int ct = 0; ct < 4; ct++) {
        acc[0][ct] = mfma(cfr[ii & 1][ct], hf[ii][0], acc[0][ct]);
        acc[1][ct] = mfma(cfr[ii & 1][ct], hf[ii][1], acc[1][ct]);
        if ((ct == 0 || ct == 2) && ii + DEPA < 16) { SB(); ldh(ii + DEPA, ct >> 1); SB(); }
      }
      SB();
      if (ii == 0) { TS(12) }
    }
  }
  TS(13)
#pragma unroll
  for (int i = 0; i < 2; i++)
#pragma unroll
    for (int e2 = 0; e2 < 2; e2++) sts128(A_TOP + w * 4096 + (i * 2 + e2) * 1024 + lane * 16, __builtin_bit_cast(u32x4, acc[i][2 + e2]));
  lds_bar();
  TS(19)
  {
    const rsrc_t rY = mkrsrc(a.out, 8388608u);
    const u32 op = (chunk * 128 + (cg >> 1) * 64 + 16 * (c >> 2) + 8 * (cg & 1) + (c & 3)) * 4096 + (s * 64 + th * 32 + 4 * g) * 4;
#pragma unroll
    for (int i = 0; i < 2; i++)
#pragma unroll
      for (int e2 = 0; e2 < 2; e2++) {
        const f32x4 o = acc[i][e2] + __builtin_bit_cast(f32x4, lds128(A_TOP + (w ^ 4) * 4096 + (i * 2 + e2) * 1024 + lane * 16));
        __builtin_amdgcn_raw_buffer_store_b128(__builtin_bit_cast(u32x4, o), rY, op + i * 16384 + e2 * 64, 0, OAUX);
      }
  }
  if (chunk == 15 && s == 0 && w == 0 && lane == 0) __builtin_amdgcn_raw_buffer_store_b32(tag, rW, WS_EPA, 0, 16);
  TS(14)
  TS_END
}

#define B_SLOT 45056u
#define B_WO 90112u
#define B_PHI 155648u
#define B_KVI (B_PHI + 1024u)
#define B_ZI (B_PHI + 3072u)

DEV void softmax2(const f32x4* p, u32* ph) {
  float mx = __builtin_fmaxf(p[0][0], p[0][1]), mn = __builtin_fminf(p[0][0], p[0][1]);
#pragma unroll
  for (int i = 2; i < 16; i += 2) {
    mx = __builtin_fmaxf(__builtin_fmaxf(mx, p[i >> 2][i & 3]), p[i >> 2][(i & 3) + 1]);
    mn = __builtin_fminf(__builtin_fminf(mn, p[i >> 2][i & 3]), p[i >> 2][(i & 3) + 1]);
  }
  const float L = 1.4426950408889634f;
  const float a = -mx * L, b = mn * L;
  f32x2 e1[8], e2[8];
#pragma unroll
  for (int i = 0; i < 8; i++) {
    const f32x2 v = (i & 1) ? hi2(p[i >> 1]) : lo2(p[i >> 1]);
    const f32x2 x1 = pfma(v, bc(L), bc(a)), x2 = pfma(v, bc(-L), bc(b));
    e1[i] = f32x2{__builtin_amdgcn_exp2f(x1.x), __builtin_amdgcn_exp2f(x1.y)};
    e2[i] = f32x2{__builtin_amdgcn_exp2f(x2.x), __builtin_amdgcn_exp2f(x2.y)};
  }
  const f32x2 s1 = ((e1[0] + e1[1]) + (e1[2] + e1[3])) + ((e1[4] + e1[5]) + (e1[6] + e1[7]));
  const f32x2 s2 = ((e2[0] + e2[1]) + (e2[2] + e2[3])) + ((e2[4] + e2[5]) + (e2[6] + e2[7]));
  const float r1 = __builtin_amdgcn_rcpf(s1.x + s1.y), r2 = __builtin_amdgcn_rcpf(s2.x + s2.y);
#pragma unroll
  for (int i = 0; i < 8; i++) {
    const f32x2 y1 = e1[i] * bc(r1), y2 = e2[i] * bc(r2);
    ph[i] = cvtpk(y1.x, y1.y);
    ph[8 + i] = cvtpk(y2.x, y2.y);
  }
}
DEV u32x4 pack_gran(f32x4 v, u32 t_lo, u32 t_hi) {
  u32x4 o;
  o.x = ((asu(v[0]) + 0x200u) & 0xFFFFFC00u) | t_lo;
  o.y = ((asu(v[1]) + 0x200u) & 0xFFFFFC00u) | t_hi;
  o.z = ((asu(v[2]) + 0x200u) & 0xFFFFFC00u) | t_lo;
  o.w = ((asu(v[3]) + 0x200u) & 0xFFFFFC00u) | t_hi;
  return o;
}
DEV u32 gran_bad(u32x4 v, u32 t_lo, u32 t_hi) { return (v.x ^ t_lo) | (v.y ^ t_hi) | (v.z ^ t_lo) | (v.w ^ t_hi); }
DEV f32x4 gran_val(u32x4 v) { return f32x4{asf(v.x & 0xFFFFFC00u), asf(v.y & 0xFFFFFC00u), asf(v.z & 0xFFFFFC00u), asf(v.w & 0xFFFFFC00u)}; }

DEV void side_b(const Args& a, const u32 lane, const u32 w, const u32 chunk, const u32 pair, const u32 tt, const u32 jc, const u32 k0) {
  TS_DECL
  const u32 g = lane >> 4, c = lane & 15;
  const u32 tg = w & 3, hh = w >> 2;
  const u32 rq = c >> 2, q = c & 3;
  const rsrc_t rX = mkrsrc(a.in[9] + chunk * 131072u, 262144u);
  const rsrc_t rQ = mkrsrc(a.in[10], 1572864u);
  const rsrc_t rP = mkrsrc(a.in[11] + pair * 512u, 1024u);
  const rsrc_t rO = mkrsrc(a.in[12], 524288u);
  const rsrc_t rW = mkrsrc(a.ws, WS_SIZE);
  u32 vx[2], vw[2], vo[2];
#pragma unroll
  for (int i = 0; i < 2; i++) {
    vx[i] = (32 * w + 8 * i + (lane >> 3)) * 1024 + ((lane & 7) ^ ((4 * i + (lane >> 4)) & 7)) * 16;
    const u32 U = 48 * (2 * w + i) + lane, k = (U >> 2) & 63;
    vw[i] = k * 3072 + (U >> 8) * 1024 + pair * 64 + ((U & 3) ^ ((k >> 2) & 2)) * 16;
    vo[i] = (64 * w + 8 * i + (lane >> 3)) * 1024 + jc * 128 + ((lane & 7) ^ (((lane >> 3) & 2) | (i << 2))) * 16;
  }
  asm volatile("s_nop 4");
  if (w == 0) glds(lane * 16, rP, 0, B_PHI);
  auto fetch = [&](int st, int i) {
    if (st < 8) {
      const u32 kb = (st + k0) & 7, d = (st % 3) * B_SLOT;
      if (i < 4) glds(vx[i & 1], rX, kb * 128 + (i >> 1) * 16384, d + w * 4096 + i * 1024);
      else glds48(vw[i - 4], rQ, kb * 196608, d + 32768 + w * 1536 + (i - 4) * 768);
    } else {
      const int n = (st - 8) * 6 + i;
      if (n < 8) glds(vo[n & 1], rO, (n >> 1) * 16384, B_WO + w * 8192 + n * 1024);
    }
  };
#pragma unroll
  for (int st = 0; st < 2; st++)
#pragma unroll
    for (int i = 0; i < 6; i++) fetch(st, i);
  const u32 tag = *(const CST4 u32*)(a.ws + WS_EPB) + 1u;
  asm volatile("" ::"s"(tag));
  TS(2)
  f32x4 qa[4], ka[4], va[4];
#pragma unroll
  for (int t = 0; t < 4; t++) { qa[t] = f32x4{0, 0, 0, 0}; ka[t] = f32x4{0, 0, 0, 0}; va[t] = f32x4{0, 0, 0, 0}; }
  const u32 ax0 = (tg * 64 + c) * 128 + (g ^ (c >> 1)) * 16;
  const u32 aw0 = 32768 + (8 * g + rq) * 64 + ((2 * hh + (q >> 1)) ^ ((g & 1) << 1)) * 16 + (q & 1) * 8;
  u32x4 aphi[4][2];
  u32x4 f0[7], f1[7];
  auto rdf = [&](u32x4* f, int st, int kk) {
    const u32 sb = (st % 3) * B_SLOT, aw = aw0 + sb + kk * 2048;
    f[5] = cat(ldstr(aw + 4096), ldstr(aw + 4096 + 256));
#pragma unroll
    for (int t = 0; t < 4; t++) f[t] = lds128((ax0 ^ (kk * 64)) + sb + t * 2048);
    f[6] = cat(ldstr(aw + 8192), ldstr(aw + 8192 + 256));
    f[4] = cat(ldstr(aw), ldstr(aw + 256));
  };
  auto mm = [&](const u32x4* f, int nst, int i0) {
#pragma unroll
    for (int t = 0; t < 4; t++) { ka[t] = mfma(f[5], f[t], ka[t]); if (t == 1) { SB(); fetch(nst, i0); SB(); } }
#pragma unroll
    for (int t = 0; t < 4; t++) { va[t] = mfma(f[t], f[6], va[t]); if (t == 1) { SB(); fetch(nst, i0 + 1); SB(); } }
#pragma unroll
    for (int t = 0; t < 4; t++) { qa[t] = mfma(f[4], f[t], qa[t]); if (t == 1) { SB(); fetch(nst, i0 + 2); SB(); } }
  };
#pragma unroll
  for (int st = 0; st < 8; st++) {
    SB();
    wait_bar<6>();
    if (st == 0) { TS(3) }
    if (st == 4) { TS(16) }
    if (st == 7) { TS(4) }
    rdf(f0, st, 0);
    SB();
    if (st > 0) mm(f1, st + 2, 0);
    else { fetch(2, 0); fetch(2, 1); fetch(2, 2); }
    SB();
    rdf(f1, st, 1);
    if (st == 0) {
#pragma unroll
      for (int i = 0; i < 4; i++) {
        const u32x2 wd = ldstr(B_PHI + hh * 512 + (4 * g + rq) * 32 + i * 8);
#pragma unroll
        for (int h = 0; h < 2; h++) {
          aphi[i][h].x = rq == 2 * h ? wd.x : 0u; aphi[i][h].y = rq == 2 * h ? wd.y : 0u;
          aphi[i][h].z = rq == 2 * h + 1 ? wd.x : 0u; aphi[i][h].w = rq == 2 * h + 1 ? wd.y : 0u;
          pin(aphi[i][h]);
        }
      }
    }
    SB();
    mm(f0, st + 2, 3);
  }
  SB();
#pragma unroll
  for (int t = 0; t < 4; t++) ka[t] = mfma(f1[5], f1[t], ka[t]);
#pragma unroll
  for (int t = 0; t < 4; t++) va[t] = mfma(f1[t], f1[6], va[t]);
#pragma unroll
  for (int t = 0; t < 4; t++) qa[t] = mfma(f1[4], f1[t], qa[t]);
  SB();
  TS(5)
  const u32 t_lo = tag & 0x3FFu, t_hi = (tag >> 10) & 0x3FFu;
  const f32x4 z4 = f32x4{0, 0, 0, 0};
  {
    const u32x4 b01 = u32x4{cvtpk(ka[0][0], ka[0][1]), cvtpk(ka[0][2], ka[0][3]), cvtpk(ka[1][0], ka[1][1]), cvtpk(ka[1][2], ka[1][3])};
    const u32x4 b23 = u32x4{cvtpk(ka[2][0], ka[2][1]), cvtpk(ka[2][2], ka[2][3]), cvtpk(ka[3][0], ka[3][1]), cvtpk(ka[3][2], ka[3][3])};
    f32x4 p[4];
#pragma unroll
    for (int i = 0; i < 4; i++) p[i] = mfma(aphi[i][0], b01, z4);
#pragma unroll
    for (int i = 0; i < 4; i++) p[i] = mfma(aphi[i][1], b23, p[i]);
    const u32x4 v01 = u32x4{cvtpk(va[0][0], va[0][1]), cvtpk(va[0][2], va[0][3]), cvtpk(va[1][0], va[1][1]), cvtpk(va[1][2], va[1][3])};
    const u32x4 v23 = u32x4{cvtpk(va[2][0], va[2][1]), cvtpk(va[2][2], va[2][3]), cvtpk(va[3][0], va[3][1]), cvtpk(va[3][2], va[3][3])};
    u32 ph[16];
    softmax2(p, ph);
    const u32 wa = w * 4096 + lane * 64, sw = (lane >> 1) & 3;
#pragma unroll
    for (int u = 0; u < 4; u++) sts128(wa + ((u ^ sw) * 16), u32x4{ph[4 * u], ph[4 * u + 1], ph[4 * u + 2], ph[4 * u + 3]});
    const u32 ra = w * 4096 + (4 * g + rq) * 64 + (((q >> 1) ^ ((2 * g + (rq >> 1)) & 3)) * 16) + (q & 1) * 8;
    const u32x4 ones = u32x4{0x3F803F80u, 0x3F803F80u, 0x3F803F80u, 0x3F803F80u};
    f32x4 kv[2], zz[2];
#pragma unroll
    for (int F = 0; F < 2; F++) {
      const u32x4 a01 = cat(ldstr((ra ^ (F * 32))), ldstr((ra ^ (F * 32)) + 1024));
      const u32x4 a23 = cat(ldstr((ra ^ (F * 32)) + 2048), ldstr((ra ^ (F * 32)) + 3072));
      kv[F] = mfma(a01, v01, z4); zz[F] = mfma(a01, ones, z4);
      kv[F] = mfma(a23, v23, kv[F]); zz[F] = mfma(a23, ones, zz[F]);
    }
#pragma unroll
    for (int F = 0; F < 2; F++) {
      sts128(w * 4096 + F * 1024 + lane * 16, __builtin_bit_cast(u32x4, kv[F]));
      if (c == 0) sts128(w * 4096 + 2048 + (F * 4 + g) * 16, __builtin_bit_cast(u32x4, zz[F]));
    }
  }
  lds_bar();
  TS(6)
  if (w < 5) {
    const u32 ra = w < 4 ? (w >> 1) * 16384 + (w & 1) * 1024 + lane * 16 : ((lane >> 3) & 1) * 16384 + 2048 + (lane & 7) * 16;
    const f32x4 sum = (__builtin_bit_cast(f32x4, lds128(ra)) + __builtin_bit_cast(f32x4, lds128(ra + 4096))) +
                      (__builtin_bit_cast(f32x4, lds128(ra + 8192)) + __builtin_bit_cast(f32x4, lds128(ra + 12288)));
    const u32 vo2 = (w < 4 || lane < 16) ? WS_GB + (pair * 8 + chunk) * 4352 + (w * 64 + lane) * 16 : OOB;
    st_sc1(pack_gran(sum, t_lo, t_hi), rW, vo2);
  }
  TS(7)
  u32x4 bq[4];
  {
    const u32x4 b01 = u32x4{cvtpk(qa[0][0], qa[0][1]), cvtpk(qa[0][2], qa[0][3]), cvtpk(qa[1][0], qa[1][1]), cvtpk(qa[1][2], qa[1][3])};
    const u32x4 b23 = u32x4{cvtpk(qa[2][0], qa[2][1]), cvtpk(qa[2][2], qa[2][3]), cvtpk(qa[3][0], qa[3][1]), cvtpk(qa[3][2], qa[3][3])};
    f32x4 p[4];
#pragma unroll
    for (int i = 0; i < 4; i++) p[i] = mfma(aphi[i][0], b01, z4);
#pragma unroll
    for (int i = 0; i < 4; i++) p[i] = mfma(aphi[i][1], b23, p[i]);
    u32 ph[16];
    softmax2(p, ph);
#pragma unroll
    for (int d = 0; d < 4; d++) {
      auto s0 = __builtin_amdgcn_permlane32_swap(ph[d], ph[8 + d], false, false);
      auto s1 = __builtin_amdgcn_permlane32_swap(ph[4 + d], ph[12 + d], false, false);
      auto t0 = __builtin_amdgcn_permlane16_swap(s0[0], s1[0], false, false);
      auto t1 = __builtin_amdgcn_permlane16_swap(s0[1], s1[1], false, false);
      bq[0][d] = t0[0]; bq[1][d] = t0[1]; bq[2][d] = t1[0]; bq[3][d] = t1[1];
    }
    pin(bq[0], bq[1], bq[2], bq[3]);
  }
  TS(8)
  {
    const u32 vg = WS_GB + (pair * 8 + (lane >> 5) * 4) * 4352 + (w * 32 + (lane & 31)) * 16;
    const u32 vz = tg == 0 ? WS_GB + (pair * 8 + (lane >> 3)) * 4352 + (256 + hh * 8 + (lane & 7)) * 16 : OOB;
    u32x4 gr[4], gz;
    const u32 zmask = tg == 0 ? 0x3FFu : 0u;
#pragma unroll 1
    for (int it = 0; it < SPIN_CAP; it++) {
      fence_c();
#pragma unroll
      for (int j = 0; j < 4; j++) gr[j] = ld_sc1(rW, vg, j * 4352);
      gz = ld_sc1(rW, vz, 0);
      const u32 bad = (((gran_bad(gr[0], t_lo, t_hi) | gran_bad(gr[1], t_lo, t_hi)) | (gran_bad(gr[2], t_lo, t_hi) | gran_bad(gr[3], t_lo, t_hi))) & 0x3FFu) |
                      (gran_bad(gz, t_lo, t_hi) & zmask);
      if (none(bad != 0)) break;
    }
    TS(9)
    f32x4 sum = (gran_val(gr[0]) + gran_val(gr[1])) + (gran_val(gr[2]) + gran_val(gr[3]));
#pragma unroll
    for (int i = 0; i < 4; i++) sum[i] = red32(sum[i]);
    if (lane < 32) sts64(B_KVI + hh * 1024 + c * 64 + ((w >> 1) & 1) * 32 + (2 * (w & 1) + g) * 8, u32x2{cvtpk(sum[0], sum[1]), cvtpk(sum[2], sum[3])});
    if (tg == 0) {
      f32x4 z = gran_val(gz);
#pragma unroll
      for (int i = 0; i < 4; i++) { z[i] += asf(dpp<0x128>(asu(z[i]))); z[i] = red32(red16(z[i])); }
      if (lane < 8) sts64(B_ZI + hh * 64 + lane * 8, u32x2{cvtpk(z[0], z[1]), cvtpk(z[2], z[3])});
    }
  }
  lds_bar();
  {
    const u32x4 akv = lds128(B_KVI + hh * 1024 + c * 64 + g * 16);
    const u32x4 az = lds128(B_ZI + hh * 64 + g * 16);
    u32 X[4][2];
#pragma unroll
    for (int t = 0; t < 4; t++) {
      const f32x4 num = mfma(akv, bq[t], z4), den = mfma(az, bq[t], z4);
      const float r = __builtin_amdgcn_rcpf(den[0] + 1e-6f);
      X[t][0] = cvtpk(num[0] * r, num[1] * r); X[t][1] = cvtpk(num[2] * r, num[3] * r);
    }
    const u32 vs = WS_O + ((chunk * 16 + tg * 4 + (g & 1)) * 16 + pair) * 1024 + (2 * hh + (g >> 1)) * 256 + c * 16;
#pragma unroll
    for (int tp = 0; tp < 2; tp++) {
      auto s0 = __builtin_amdgcn_permlane16_swap(X[2 * tp][0], X[2 * tp + 1][0], false, false);
      auto s1 = __builtin_amdgcn_permlane16_swap(X[2 * tp][1], X[2 * tp + 1][1], false, false);
      st_sc1(u32x4{s0[0], s1[0], s0[1], s1[1]}, rW, vs + tp * 32768);
    }
  }
  TS(10)
  wait_bar<0>();
  if (w == 0 && lane < 16) __builtin_amdgcn_raw_buffer_store_b64(u32x2{tag, tag}, rW, WS_FLB + (chunk * 16 + lane) * 128 + pair * 8, 0, 16);
  TS(11)
  const u32 wb = B_WO + hh * 32768 + (8 * g + rq) * 128 + (((q >> 1) ^ ((rq & 2) | ((g & 1) << 2))) * 16) + (q & 1) * 8;
  u32x4 wfr[2][4];
  auto rdw = [&](int ii) {
    const u32 y = ((ii + jc) & 7) * 4096;
#pragma unroll
    for (int ct = 0; ct < 4; ct++) {
      const u32 ad = (wb ^ ((ct ^ (2 * hh)) * 32)) + y;
      wfr[ii & 1][ct] = cat(ldstr(ad), ldstr(ad + 512));
    }
  };
  rdw(0);
  {
    const u32 vf = WS_FLB + (tt * 8 + jc) * 128 + (hh * 8 + (lane & 7)) * 8;
#pragma unroll 1
    for (int it = 0; it < SPIN_CAP; it++) {
      fence_c();
      const u32x2 f = ld64_sc1(rW, vf, 0);
      if (none(((f.x ^ tag) | (f.y ^ tag)) != 0)) break;
    }
  }
  TS(12)
  f32x4 acc[2][4];
#pragma unroll
  for (int i = 0; i < 2; i++)
#pragma unroll
    for (int ct = 0; ct < 4; ct++) acc[i][ct] = z4;
  {
    u32x4 of[8][2];
    const u32 vh = WS_O + ((tt * 8 + 2 * tg) * 16 + hh * 8) * 1024 + lane * 16;
    auto ldo = [&](int ii, int i) { of[ii][i] = ld_sc1(rW, vh, ((ii + jc) & 7) * 1024 + i * 16384); };
#pragma unroll
    for (int ii = 0; ii < DEPB; ii++) { ldo(ii, 0); ldo(ii, 1); }
    SB();
#pragma unroll
    for (int ii = 0; ii < 8; ii++) {
      if (ii < 7) rdw(ii + 1);
      SB();
#pragma unroll
      for (int ct = 0; ct < 4; ct++) {
        acc[0][ct] = mfma(wfr[ii & 1][ct], of[ii][0], acc[0][ct]);
        acc[1][ct] = mfma(wfr[ii & 1][ct], of[ii][1], acc[1][ct]);
        if ((ct == 0 || ct == 2) && ii + DEPB < 8) { SB(); ldo(ii + DEPB, ct >> 1); SB(); }
      }
      SB();
      if (ii == 0) { TS(13) }
    }
  }
  TS(14)
#pragma unroll
  for (int i = 0; i < 2; i++)
#pragma unroll
    for (int e2 = 0; e2 < 2; e2++) sts128(w * 4096 + (i * 2 + e2) * 1024 + lane * 16, __builtin_bit_cast(u32x4, acc[i][2 + e2]));
  lds_bar();
  {
    const rsrc_t rY = mkrsrc(a.out, 8388608u);
    const u32 op = (tt * 128 + tg * 32 + c) * 4096 + (512 + jc * 64 + hh * 32 + 4 * g) * 4;
#pragma unroll
    for (int i = 0; i < 2; i++)
#pragma unroll
      for (int e2 = 0; e2 < 2; e2++) {
        const f32x4 o = acc[i][e2] + __builtin_bit_cast(f32x4, lds128((w ^ 4) * 4096 + (i * 2 + e2) * 1024 + lane * 16));
        const u32 p0 = cvtpk(o[0], o[1]), p1 = cvtpk(o[2], o[3]);
        __builtin_amdgcn_raw_buffer_store_b128(u32x4{p0 << 16, p0 & 0xffff0000u, p1 << 16, p1 & 0xffff0000u}, rY, op + i * 65536 + e2 * 64, 0, OAUX);
      }
  }
  if (tt == 0 && jc == 0 && w == 0 && lane == 0) __builtin_amdgcn_raw_buffer_store_b32(tag, rW, WS_EPB, 0, 16);
  TS(15)
  TS_END
}

__global__ __launch_bounds__(512, 2) void fused_s5_hedgehog(const Args a) {
  __shared__ __attribute__((aligned(1024))) char smem[163840];
  asm volatile("" ::"s"((u32)(size_t)(LDS3 char*)smem) : "memory");
  const u32 b = blockIdx.x, xcd = b & 7, j = b >> 3;
  const u32 lane = threadIdx.x & 63, w = rfl(threadIdx.x >> 6);
  if (xcd < 4) {
    const u32 k0 = ROTA == 0 ? 0u : ROTA == 1 ? 4u * ((j ^ (j >> 3)) & 1) : ((j + 2 * (j >> 3)) & 7);
    side_a(a, lane, w, xcd * 4 + (j >> 3), j & 7, k0);
  } else {
    const u32 x = xcd - 4;
    const u32 k0 = ROTB == 0 ? 0u : ROTB == 1 ? 4u * ((j ^ (j >> 3)) & 1) : ((j + 2 * (j >> 3)) & 7);
    side_b(a, lane, w, (x & 1) * 4 + (j >> 3), (x >> 1) * 8 + (j & 7), x * 4 + (j >> 3), j & 7, k0);
  }
}

__device__ u64 metric(const u64* ts, int kind, int idx, u64* sm){
  int t=threadIdx.x; u64 mn=~0ull, mxs=0, mxe=0, acc=0;
  for(int b=t;b<2048;b+=256){ mn=min(mn,ts[b*NTS]); mxs=max(mxs,ts[b*NTS]); mxe=max(mxe,ts[b*NTS+NTS-1]); }
  sm[t]=mn; sm[256+t]=mxs; sm[512+t]=mxe; __syncthreads();
  for(int i=0;i<256;i++){ mn=min(mn,sm[i]); mxs=max(mxs,sm[256+i]); mxe=max(mxe,sm[512+i]); }
  __syncthreads();
  for(int b=t;b<2048;b+=256){ bool A=((b>>3)&7)<4;
    if(kind==0&&A) acc+=ts[b*NTS+idx]-ts[b*NTS+1]; if(kind==1&&!A) acc+=ts[b*NTS+idx]-ts[b*NTS+1];
    if(kind==4&&A) acc+=ts[b*NTS+NTS-1]-mn; if(kind==5&&!A) acc+=ts[b*NTS+NTS-1]-mn; if(kind==6&&A) acc+=ts[b*NTS]-mn; if(kind==7&&!A) acc+=ts[b*NTS]-mn; }
  sm[t]=acc; __syncthreads(); acc=0; for(int i=0;i<256;i++) acc+=sm[i]; __syncthreads();
  if(kind<2) return (acc/1024+125)/250;
  { const u64* x=ts+2048*NTS; if(kind==8) return ((mn-x[1])*8+50)/100; if(kind==9) return ((x[2]-mxe)*8+50)/100; if(kind==10) return ((x[1]-x[0])*8+50)/100; if(kind==11) return ((x[3]-x[2])*8+50)/100; }
  if(kind==2) return ((mxe-mn)*8+50)/100; if(kind==3) return ((mxs-mn)*8+50)/100;
  return ((acc/128)+50)/100;
}
__global__ void stampk(char* ws,int i){ u64 t=rt_(); if(threadIdx.x==0) ((u64*)(ws+WS_TS))[2048*NTS+i]=t; }
__global__ void prn(const char* ws){
  __shared__ u64 sm[768];
  const u64* ts=(const u64*)(ws+WS_TS);
  u64 a=min(metric(ts,K1,I1,sm),(u64)199), b=min(metric(ts,K2,I2,sm),(u64)199);
  if(threadIdx.x==0){ u64 v=(a*200+b)*400; u64 t0=rt_(); while(rt_()-t0<v) __builtin_amdgcn_s_sleep(8); }
}
extern "C" void kernel_launch(void* const* d_in, const int* in_sizes, int n_in, void* d_out, int out_size, void* d_ws,
                              size_t ws_size, hipStream_t stream) {
  Args a;
  for (int i = 0; i < 13; i++) a.in[i] = (const u16*)d_in[i];
  a.out = (float*)d_out;
  a.ws = (char*)d_ws;
  stampk<<<1,64,0,stream>>>((char*)d_ws,0); stampk<<<1,64,0,stream>>>((char*)d_ws,1);
  fused_s5_hedgehog<<<256, 512, 0, stream>>>(a);
  stampk<<<1,64,0,stream>>>((char*)d_ws,2); stampk<<<1,64,0,stream>>>((char*)d_ws,3);
  prn<<<1,256,0,stream>>>((const char*)d_ws);
}
```

```cpp
#include <hip/hip_runtime.h>
#pragma clang diagnostic ignored "-Winline-asm"

typedef unsigned short u16;
typedef unsigned int u32;
typedef unsigned long long u64;
typedef float f32x4 __attribute__((ext_vector_type(4)));
typedef u32 u32x4 __attribute__((ext_vector_type(4)));
typedef u32 u32x2 __attribute__((ext_vector_type(2)));
typedef __bf16 bf16x8 __attribute__((ext_vector_type(8)));
typedef short s16x4 __attribute__((ext_vector_type(4)));
typedef __amdgpu_buffer_rsrc_t rsrc_t;
#define LDS3 __attribute__((address_space(3)))
#define CST4 __attribute__((address_space(4)))
#define DEV __device__ __forceinline__

#define ROTA 1
#define ROTB 1
#define OAUX 16
#define DEPA 5
#define DEPB 5

#define WS_EPA 0u
#define WS_EPB 256u
#define WS_FLA 4096u
#define WS_FLB 20480u
#define WS_GA 65536u
#define WS_GB 196608u
#define WS_H 1048576u
#define WS_O 5242880u
#define WS_SIZE 7340032u
#define WS_TS 8388608u
#define OOB 0xFFFFFF00u
#define SPIN_CAP (1 << 20)

struct Args {
  const u16* in[13];
  float* out;
  char* ws;
};

DEV u32 rfl(u32 x) { return __builtin_amdgcn_readfirstlane(x); }
DEV rsrc_t mkrsrc(const void* p, u32 bytes) { return __builtin_amdgcn_make_buffer_rsrc((void*)p, (short)0, (int)bytes, 0x00020000); }
DEV void glds(u32 voff, rsrc_t srd, u32 soff, u32 ldst) {
  asm volatile("s_mov_b32 m0, %0\n\ts_nop 0\n\tbuffer_load_dwordx4 %1, %2, %3 offen lds" ::"s"(ldst), "v"(voff), "s"(srd), "s"(soff) : "m0", "memory");
}
DEV void glds48(u32 voff, rsrc_t srd, u32 soff, u32 ldst) {
  asm volatile("s_mov_b32 m0, %0\n\ts_mov_b32 exec_hi, 0xffff\n\tbuffer_load_dwordx4 %1, %2, %3 offen lds\n\ts_mov_b32 exec_hi, -1" ::"s"(ldst), "v"(voff), "s"(srd), "s"(soff) : "m0", "memory");
}
template <int N> DEV void wait_bar() { asm volatile("s_waitcnt vmcnt(%0) lgkmcnt(0)\n\ts_barrier" ::"n"(N) : "memory"); }
DEV void lds_bar() { asm volatile("s_waitcnt lgkmcnt(0)\n\ts_barrier" ::: "memory"); }
DEV void raw_bar() { asm volatile("s_barrier" ::: "memory"); }
DEV void fence_c() { asm volatile("" ::: "memory"); }
DEV u32x4 lds128(u32 a) { return *reinterpret_cast<const LDS3 u32x4*>(a); }
DEV u32x2 lds64(u32 a) { return *reinterpret_cast<const LDS3 u32x2*>(a); }
DEV u32 lds16(u32 a) { return *reinterpret_cast<const LDS3 u16*>(a); }
DEV void sts128(u32 a, u32x4 v) { *reinterpret_cast<LDS3 u32x4*>(a) = v; }
DEV void sts64(u32 a, u32x2 v) { *reinterpret_cast<LDS3 u32x2*>(a) = v; }
DEV u32x2 ldstr(u32 a) { return __builtin_bit_cast(u32x2, __builtin_amdgcn_ds_read_tr16_b64_v4i16(reinterpret_cast<LDS3 s16x4*>(a))); }
DEV f32x4 mfma(u32x4 a, u32x4 b, f32x4 c) {
  return __builtin_amdgcn_mfma_f32_16x16x32_bf16(__builtin_bit_cast(bf16x8, a), __builtin_bit_cast(bf16x8, b), c, 0, 0, 0);
}
typedef float f32x2 __attribute__((ext_vector_type(2)));
typedef __bf16 bf16x2 __attribute__((ext_vector_type(2)));
DEV u32 cvtpk(float lo, float hi) { return __builtin_bit_cast(u32, __builtin_convertvector(f32x2{lo, hi}, bf16x2)); }
DEV float asf(u32 x) { return __uint_as_float(x); }
DEV u32 asu(float x) { return __float_as_uint(x); }
DEV float bflo(u32 x) { return asf(x << 16); }
DEV float bfhi(u32 x) { return asf(x & 0xffff0000u); }
DEV u32x4 cat(u32x2 a, u32x2 b) { u32x4 r; r.x = a.x; r.y = a.y; r.z = b.x; r.w = b.y; return r; }
DEV u32x4 ld_sc1(rsrc_t r, u32 voff, u32 soff) { return __builtin_amdgcn_raw_buffer_load_b128(r, voff, soff, 16); }
DEV u32x2 ld64_sc1(rsrc_t r, u32 voff, u32 soff) { return __builtin_amdgcn_raw_buffer_load_b64(r, voff, soff, 16); }
DEV u32x4 ld_pl(rsrc_t r, u32 voff, u32 soff) { return __builtin_amdgcn_raw_buffer_load_b128(r, voff, soff, 0); }
DEV void st_sc1(u32x4 v, rsrc_t r, u32 voff) { __builtin_amdgcn_raw_buffer_store_b128(v, r, voff, 0, 16); }
DEV void st32_sc1(u32 v, rsrc_t r, u32 voff, u32 soff) { __builtin_amdgcn_raw_buffer_store_b32(v, r, voff, soff, 16); }
#define SB() __builtin_amdgcn_sched_barrier(0)
template <class T> DEV void pin(T& x) { asm volatile("" : "+v"(x)); }
template <class T, class... R> DEV void pin(T& x, R&... r) { pin(x); pin(r...); }
DEV f32x2 pfma(f32x2 a, f32x2 b, f32x2 c) { return __builtin_elementwise_fma(a, b, c); }
DEV f32x2 bc(float x) { return f32x2{x, x}; }
DEV f32x2 lo2(f32x4 v) { return __builtin_shufflevector(v, v, 0, 1); }
DEV f32x2 hi2(f32x4 v) { return __builtin_shufflevector(v, v, 2, 3); }
DEV bool none(bool bad) { return __builtin_amdgcn_ballot_w64(bad) == 0; }
DEV float red32(float x) { auto r = __builtin_amdgcn_permlane32_swap(asu(x), asu(x), false, false); return asf(r[0]) + asf(r[1]); }
DEV float red16(float x) { auto r = __builtin_amdgcn_permlane16_swap(asu(x), asu(x), false, false); return asf(r[0]) + asf(r[1]); }
template <int CTRL> DEV u32 dpp(u32 x) { return __builtin_amdgcn_update_dpp(0u, x, CTRL, 0xf, 0xf, true); }
struct cf { float r, i; };
DEV void pinc(cf& x) { asm volatile("" : "+v"(x.r), "+v"(x.i)); }
DEV cf cmul(cf a, cf b) { cf o; o.r = a.r * b.r - a.i * b.i; o.i = a.r * b.i + a.i * b.r; return o; }
DEV cf cfma(cf a, cf b, cf c) {
  cf o; o.r = __builtin_fmaf(a.r, b.r, c.r); o.i = __builtin_fmaf(a.r, b.i, c.i);
  o.r = __builtin_fmaf(-a.i, b.i, o.r); o.i = __builtin_fmaf(a.i, b.r, o.i); return o;
}

#define TS_DECL
#define TS(i)
#define TS_END

#define A_TOP 131072u
#define A_EH (A_TOP + 1024u)

DEV cf csel(u32 g, cf a0, cf a1, cf a2, cf a3) {
  cf o;
  o.r = g == 0 ? a0.r : g == 1 ? a1.r : g == 2 ? a2.r : a3.r;
  o.i = g == 0 ? a0.i : g == 1 ? a1.i : g == 2 ? a2.i : a3.i;
  return o;
}

DEV void side_a(const Args& a, const u32 lane, const u32 w, const u32 chunk, const u32 s, const u32 k0) {
  TS_DECL
  const u32 g = lane >> 4, c = lane & 15;
  const u32 th = w >> 2, cg = w & 3;
  u32 p_lr, p_li, p_dt;
  u32x2 dsk[2];
  {
    const u32 o1 = (s * 64 + cg * 16 + c) * 2, o2 = s * 128 + th * 64 + g * 8;
    asm volatile("global_load_ushort %0, %1, %2" : "=v"(p_lr) : "v"(o1), "s"(a.in[1]) : "memory");
    asm volatile("global_load_ushort %0, %1, %2" : "=v"(p_li) : "v"(o1), "s"(a.in[2]) : "memory");
    asm volatile("global_load_ushort %0, %1, %2" : "=v"(p_dt) : "v"(o1), "s"(a.in[7]) : "memory");
    asm volatile("global_load_dwordx2 %0, %1, %2" : "=v"(dsk[0]) : "v"(o2), "s"(a.in[8]) : "memory");
    asm volatile("global_load_dwordx2 %0, %1, %2 offset:32" : "=v"(dsk[1]) : "v"(o2), "s"(a.in[8]) : "memory");
  }
  const rsrc_t rX = mkrsrc(a.in[0] + chunk * 65536u, 131072u);
  const rsrc_t rB = mkrsrc((w < 4 ? a.in[3] : a.in[4]) + s * 32768u, 65536u);
  const rsrc_t rCr = mkrsrc(a.in[5] + s * 32768u, 65536u);
  const rsrc_t rCi = mkrsrc(a.in[6] + s * 32768u, 65536u);
  const rsrc_t rW = mkrsrc(a.ws, WS_SIZE);
  u32 vx[2], vb[2];
#pragma unroll
  for (int i = 0; i < 2; i++) {
    const u32 rho = 8 * (2 * w + i) + (lane >> 3);
    const u32 unit = (lane & 7) ^ ((rho >> 1) & 7);
    const u32 tok = (rho & 64) + 16 * ((rho >> 2) & 3) + 4 * ((rho >> 4) & 3) + (rho & 3);
    vx[i] = tok * 1024 + unit * 16;
    vb[i] = (rho & 63) * 1024 + unit * 16;
  }
  const u32 vc = (8 * w + (lane >> 3)) * 1024 + (lane & 7) * 16;
  u32x4 cw[4][4];
  auto fetch = [&](int st, int i) {
    if (st < 8) {
      const u32 so = ((st + k0) & 7) * 128, d = (st & 3) * 32768 + w * 2048;
      if (i < 2) glds(vx[i], rX, so, d + i * 1024);
      else glds(vb[i - 2], rB, so, d + 16384 + (i - 2) * 1024);
    } else {
      cw[st - 8][i] = ld_pl((i & 1) ? rCi : rCr, vc, (st - 8) * 256 + (i >> 1) * 128);
    }
  };
#pragma unroll
  for (int st = 0; st < 3; st++)
#pragma unroll
    for (int i = 0; i < 4; i++) fetch(st, i);
  const u32 tag = *(const CST4 u32*)(a.ws + WS_EPA) + 1u;
  asm volatile("" ::"s"(tag));
  TS(2)
  asm volatile("s_waitcnt vmcnt(12)" : "+v"(p_lr), "+v"(p_li), "+v"(p_dt), "+v"(dsk[0]), "+v"(dsk[1])::"memory");
  cf lam, l2, lam16, lam64, c2, wg, wl[4];
  {
    const float L2E = 1.4426950408889634f;
    const float lr = bflo(p_lr), li = bflo(p_li);
    const float dt = bflo(cvtpk(__builtin_amdgcn_exp2f(bflo(p_dt) * L2E), 0.f));
    const float e = __builtin_amdgcn_exp2f(dt * lr * L2E);
    const float rev = __builtin_amdgcn_fractf(dt * li * 0.15915494309189535f);
    lam.r = e * __builtin_amdgcn_cosf(rev); lam.i = e * __builtin_amdgcn_sinf(rev);
    const float nr = lam.r - 1.f, ni = lam.i, rd = 2.f * __builtin_amdgcn_rcpf(lr * lr + li * li);
    c2.r = (nr * lr + ni * li) * rd; c2.i = (ni * lr - nr * li) * rd;
    l2 = cmul(lam, lam);
    const cf l4 = cmul(l2, l2), l8 = cmul(l4, l4);
    lam16 = cmul(l8, l8);
    const cf l32 = cmul(lam16, lam16), l48 = cmul(l32, lam16);
    lam64 = cmul(l32, l32);
    const cf mu = cmul(lam64, lam64), mu2 = cmul(mu, mu), mu3 = cmul(mu2, mu), mu4 = cmul(mu2, mu2);
    wg = csel(g, cf{1.f, 0.f}, lam16, l32, l48);
    wl[0] = csel(g, cf{1.f, 0.f}, mu, mu2, mu3);
    wl[1] = cmul(wl[0], mu4); wl[2] = cmul(wl[1], mu4); wl[3] = cmul(wl[2], mu4);
    pinc(lam); pinc(l2); pinc(lam16); pinc(lam64); pinc(c2); pinc(wg); pinc(wl[0]); pinc(wl[1]); pinc(wl[2]); pinc(wl[3]);
  }
  u32 vo[4], need = 0;
#pragma unroll
  for (int j = 0; j < 4; j++) {
    const int i = (int)chunk - 1 - (int)g - 4 * j;
    vo[j] = i >= 0 ? WS_GA + ((i * 8 + s) * 64 + cg * 16 + c) * 16 : OOB;
    need |= (i >= 0 ? 1u : 0u) << j;
  }
  u32 vga = WS_GA + ((chunk * 8 + s) * 64 + cg * 16 + c) * 16, aeh = A_EH + (cg * 16 + c) * 8;
  u32 vhs = WS_H + ((chunk * 8 + th * 4) * 32 + s * 4 + cg) * 1024 + g * 256 + c * 4;
  pin(vo[0], vo[1], vo[2], vo[3], need, vga, aeh, vhs);
  f32x4 ur[4], ui[4];
#pragma unroll
  for (int t = 0; t < 4; t++) { ur[t] = f32x4{0, 0, 0, 0}; ui[t] = f32x4{0, 0, 0, 0}; }
  const u32 ax0 = (th * 64 + c) * 128 + (g ^ (c >> 1)) * 16;
  const u32 ab0 = 16384 + (cg * 16 + c) * 128 + (g ^ (c >> 1)) * 16;
  const u32 st_skip = (s - k0) & 7;
  const u32 axs = (32 * cg + c) * 128 + ((4 * th + (g >> 1)) ^ (c >> 1)) * 16 + (g & 1) * 8;
  u32x2 xs[2][2];
#pragma unroll
  for (int i = 0; i < 2; i++) { xs[i][0] = u32x2{0, 0}; xs[i][1] = u32x2{0, 0}; }
  u32x4 f0[6], f1[6];
  auto rdf = [&](u32x4* f, int st, int kk) {
    const u32 sb = (st & 3) * 32768;
    f[4] = lds128((ab0 ^ (kk * 64)) + sb);
    f[0] = lds128((ax0 ^ (kk * 64)) + sb);
    f[5] = lds128((ab0 ^ (kk * 64)) + sb + 8192);
#pragma unroll
    for (int t = 1; t < 4; t++) f[t] = lds128((ax0 ^ (kk * 64)) + sb + t * 2048);
  };
  auto mm = [&](const u32x4* f, int nst, int i0) {
#pragma unroll
    for (int t = 0; t < 4; t++) {
      ur[t] = mfma(f[t], f[4], ur[t]);
      ui[t] = mfma(f[t], f[5], ui[t]);
      if (t == 0 || t == 2) { SB(); fetch(nst, i0 + (t >> 1)); SB(); }
    }
  };
#pragma unroll
  for (int st = 0; st < 8; st++) {
    SB();
    wait_bar<8>();
    if (st == 0) { TS(3) }
    if (st == 4) { TS(16) }
    if (st == 7) { TS(4) }
    rdf(f0, st, 0);
    SB();
    if (st > 0) mm(f1, st + 3, 0);
    else { fetch(3, 0); fetch(3, 1); }
    SB();
    rdf(f1, st, 1);
    if (st_skip == (u32)st) {
      const u32 sb = (st & 3) * 32768;
#pragma unroll
      for (int i = 0; i < 2; i++) { xs[i][0] = lds64(axs + sb + i * 2048); fence_c(); xs[i][1] = lds64((axs ^ 32) + sb + i * 2048); fence_c(); }
    }
    SB();
    mm(f0, st + 3, 2);
  }
  SB();
  mm(f1, 11, 0);
  SB();
  fetch(11, 2); fetch(11, 3);
  TS(5)
  cf e;
  {
    f32x2 er = lo2(ur[0]), ei = lo2(ui[0]);
#pragma unroll
    for (int k = 1; k < 8; k++) {
      const f32x2 R = (k & 1) ? hi2(ur[k >> 1]) : lo2(ur[k >> 1]), I = (k & 1) ? hi2(ui[k >> 1]) : lo2(ui[k >> 1]);
      const f32x2 nr = pfma(bc(-l2.i), ei, pfma(bc(l2.r), er, R));
      ei = pfma(bc(l2.i), er, pfma(bc(l2.r), ei, I));
      er = nr;
    }
    e = cfma(lam, cf{er.x, ei.x}, cf{er.y, ei.y});
  }
  cf eg[4];
  {
    auto r = __builtin_amdgcn_permlane32_swap(asu(e.r), asu(e.r), false, false);
    auto q = __builtin_amdgcn_permlane32_swap(asu(e.i), asu(e.i), false, false);
    auto r0 = __builtin_amdgcn_permlane16_swap(r[0], r[0], false, false);
    auto q0 = __builtin_amdgcn_permlane16_swap(q[0], q[0], false, false);
    auto r1 = __builtin_amdgcn_permlane16_swap(r[1], r[1], false, false);
    auto q1 = __builtin_amdgcn_permlane16_swap(q[1], q[1], false, false);
    eg[0].r = asf(r0[0]); eg[1].r = asf(r0[1]); eg[2].r = asf(r1[0]); eg[3].r = asf(r1[1]);
    eg[0].i = asf(q0[0]); eg[1].i = asf(q0[1]); eg[2].i = asf(q1[0]); eg[3].i = asf(q1[1]);
  }
  const cf s2 = cfma(lam16, eg[0], eg[1]), s3 = cfma(lam16, s2, eg[2]), eh = cfma(lam16, s3, eg[3]);
  if (th == 0 && g == 0) sts64(aeh, u32x2{asu(eh.r), asu(eh.i)});
  lds_bar();
  cf pre = csel(g, cf{0.f, 0.f}, eg[0], s2, s3);
  if (th == 1) {
    const u32x2 t = lds64(aeh);
    const cf e0{asf(t.x), asf(t.y)};
    const cf tot = cfma(lam64, e0, eh);
    if (g == 0) st_sc1(u32x4{asu(tot.r), tag, asu(tot.i), tag}, rW, vga);
    pre = cfma(wg, e0, pre);
    wg = cmul(wg, lam64);
  }
  TS(6)
  SB();
  f32x2 hr[8], hi[8], wr[8], wi[8];
  {
    const cf p2 = cmul(c2, pre);
    wg = cmul(c2, wg);
    cf h{p2.r, -p2.i};
    const cf lc{lam.r, -lam.i};
#pragma unroll
    for (int k = 0; k < 8; k++) {
      const f32x2 R = (k & 1) ? hi2(ur[k >> 1]) : lo2(ur[k >> 1]), I = (k & 1) ? hi2(ui[k >> 1]) : lo2(ui[k >> 1]);
      const f32x2 vr = pfma(bc(-c2.i), I, bc(c2.r) * R), vi = pfma(bc(-c2.i), R, bc(-c2.r) * I);
      const cf h0 = cfma(lc, h, cf{vr.x, vi.x});
      h = cfma(lc, h0, cf{vr.y, vi.y});
      hr[k] = f32x2{h0.r, h.r}; hi[k] = f32x2{h0.i, h.i};
    }
    wr[0] = f32x2{lam.r, l2.r}; wi[0] = f32x2{-lam.i, -l2.i};
#pragma unroll
    for (int k = 1; k < 8; k++) {
      wr[k] = pfma(bc(l2.i), wi[k - 1], bc(l2.r) * wr[k - 1]);
      wi[k] = pfma(bc(-l2.i), wr[k - 1], bc(l2.r) * wi[k - 1]);
    }
#pragma unroll
    for (int k = 0; k < 8; k++) pin(hr[k], hi[k], wr[k], wi[k]);
    pinc(wg);
  }
  SB();
  u32x4 gr[4];
#pragma unroll
  for (int j = 0; j < 4; j++) gr[j] = ld_sc1(rW, vo[j], 0);
  SB();
  TS(17)
  f32x4 acc[2][4];
#pragma unroll
  for (int i = 0; i < 2; i++) {
#pragma unroll
    for (int e2 = 0; e2 < 2; e2++) {
      const u32 p0 = cvtpk(bflo(xs[i][e2].x) * bflo(dsk[e2].x), bfhi(xs[i][e2].x) * bfhi(dsk[e2].x));
      const u32 p1 = cvtpk(bflo(xs[i][e2].y) * bflo(dsk[e2].y), bfhi(xs[i][e2].y) * bfhi(dsk[e2].y));
      acc[i][e2] = f32x4{bflo(p0), bfhi(p0), bflo(p1), bfhi(p1)};
      acc[i][2 + e2] = f32x4{0, 0, 0, 0};
      pin(acc[i][e2]);
    }
  }
  {
    const u32 u = lane & 7, pb = ((u >> 1) - 2 * s) & 15;
    const u32 rest = (w >> 1) * 1024 + (u & 1) * 512 + ((8 * (w & 1) + (lane >> 3)) ^ (2 * (pb & 3) + (u & 1))) * 16;
#pragma unroll
    for (int p = 0; p < 4; p++)
#pragma unroll
      for (int hf = 0; hf < 2; hf++) {
        const u32 ad = ((((pb + (p & 1) * 8 + hf * 4) & 15) << 12) | (rest ^ ((p >> 1) * 2048))) + (p >> 1) * 65536;
        const u32x4 R = cw[p][2 * hf], I = cw[p][2 * hf + 1];
        sts128(ad, u32x4{__builtin_amdgcn_perm(I.x, R.x, 0x05040100u), __builtin_amdgcn_perm(I.x, R.x, 0x07060302u),
                         __builtin_amdgcn_perm(I.y, R.y, 0x05040100u), __builtin_amdgcn_perm(I.y, R.y, 0x07060302u)});
        sts128(ad + 256, u32x4{__builtin_amdgcn_perm(I.z, R.z, 0x05040100u), __builtin_amdgcn_perm(I.z, R.z, 0x07060302u),
                               __builtin_amdgcn_perm(I.w, R.w, 0x05040100u), __builtin_amdgcn_perm(I.w, R.w, 0x07060302u)});
      }
  }
  SB();
  TS(18)
  {
#pragma unroll 1
    for (int it = 0; it < SPIN_CAP; it++) {
      u32 have = 0;
#pragma unroll
      for (int j = 0; j < 4; j++) have |= (((gr[j].y ^ tag) | (gr[j].w ^ tag)) == 0 ? 1u : 0u) << j;
      if (none((need & ~have) != 0)) break;
      fence_c();
#pragma unroll
      for (int j = 0; j < 4; j++) gr[j] = ld_sc1(rW, vo[j], 0);
    }
  }
  TS(7)
  {
    cf H = cmul(wl[0], cf{asf(gr[0].x), asf(gr[0].z)});
#pragma unroll
    for (int j = 1; j < 4; j++) H = cfma(wl[j], cf{asf(gr[j].x), asf(gr[j].z)}, H);
    H.r = red16(red32(H.r)); H.i = red16(red32(H.i));
    const cf z = cmul(wg, H);
#pragma unroll
    for (int k = 0; k < 8; k++) {
      const f32x2 yr = pfma(wi[k], bc(z.i), pfma(wr[k], bc(z.r), hr[k]));
      const f32x2 yi = pfma(wr[k], bc(-z.i), pfma(wi[k], bc(z.r), hi[k]));
      st32_sc1(cvtpk(yr.x, yi.x), rW, vhs + (k & 1) * 128, (k >> 1) * 32768);
      st32_sc1(cvtpk(yr.y, yi.y), rW, vhs + (k & 1) * 128 + 64, (k >> 1) * 32768);
    }
  }
  TS(8)
  wait_bar<0>();
  if (w == 0 && lane < 8) __builtin_amdgcn_raw_buffer_store_b64(u32x2{tag, tag}, rW, WS_FLA + (chunk * 8 + lane) * 128 + s * 8, 0, 16);
  TS(9)
  u32 cb[4];
#pragma unroll
  for (int i = 0; i < 4; i++) cb[i] = th * 65536 + g * 256 + ((c ^ (2 * i + (g >> 1))) * 16);
  u32x4 cfr[2][4];
  auto rdc = [&](int ii) {
#pragma unroll
    for (int ct = 0; ct < 4; ct++) cfr[ii & 1][ct] = lds128(cb[ii & 3] + ii * 4096 + ct * 1024);
  };
  rdc(0);
  {
    const u32 vf = WS_FLA + (chunk * 8 + s) * 128 + (th * 4 + (lane & 3)) * 8;
#pragma unroll 1
    for (int it = 0; it < SPIN_CAP; it++) {
      fence_c();
      const u32x2 f = ld64_sc1(rW, vf, 0);
      if (none(((f.x ^ tag) | (f.y ^ tag)) != 0)) break;
    }
  }
  TS(10)
  {
    u32x4 hf[16][2];
    const u32 vh = WS_H + ((chunk * 8 + 2 * cg) * 32 + th * 16) * 1024 + c * 64 + g * 16;
    auto ldh = [&](int ii, int i) { hf[ii][i] = ld_sc1(rW, vh, ((ii + 2 * s) & 15) * 1024 + i * 32768); };
#pragma unroll
    for (int ii = 0; ii < DEPA; ii++) { ldh(ii, 0); ldh(ii, 1); }
    SB();
    TS(11)
#pragma unroll
    for (int ii = 0; ii < 16; ii++) {
      if (ii < 15) rdc(ii + 1);
      SB();
#pragma unroll
      for (int ct = 0; ct < 4; ct++) {
        acc[0][ct] = mfma(cfr[ii & 1][ct], hf[ii][0], acc[0][ct]);
        acc[1][ct] = mfma(cfr[ii & 1][ct], hf[ii][1], acc[1][ct]);
        if ((ct == 0 || ct == 2) && ii + DEPA < 16) { SB(); ldh(ii + DEPA, ct >> 1); SB(); }
      }
      SB();
      if (ii == 0) { TS(12) }
    }
  }
  TS(13)
#pragma unroll
  for (int i = 0; i < 2; i++)
#pragma unroll
    for (int e2 = 0; e2 < 2; e2++) sts128(A_TOP + w * 4096 + (i * 2 + e2) * 1024 + lane * 16, __builtin_bit_cast(u32x4, acc[i][2 + e2]));
  lds_bar();
  TS(19)
  {
    const rsrc_t rY = mkrsrc(a.out, 8388608u);
    const u32 op = (chunk * 128 + (cg >> 1) * 64 + 16 * (c >> 2) + 8 * (cg & 1) + (c & 3)) * 4096 + (s * 64 + th * 32 + 4 * g) * 4;
#pragma unroll
    for (int i = 0; i < 2; i++)
#pragma unroll
      for (int e2 = 0; e2 < 2; e2++) {
        const f32x4 o = acc[i][e2] + __builtin_bit_cast(f32x4, lds128(A_TOP + (w ^ 4) * 4096 + (i * 2 + e2) * 1024 + lane * 16));
        __builtin_amdgcn_raw_buffer_store_b128(__builtin_bit_cast(u32x4, o), rY, op + i * 16384 + e2 * 64, 0, OAUX);
      }
  }
  if (chunk == 15 && s == 0 && w == 0 && lane == 0) __builtin_amdgcn_raw_buffer_store_b32(tag, rW, WS_EPA, 0, 16);
  TS(14)
  TS_END
}

#define B_SLOT 45056u
#define B_WO 90112u
#define B_PHI 155648u
#define B_KVI (B_PHI + 1024u)
#define B_ZI (B_PHI + 3072u)

DEV void softmax2(const f32x4* p, u32* ph) {
  float mx = __builtin_fmaxf(p[0][0], p[0][1]), mn = __builtin_fminf(p[0][0], p[0][1]);
#pragma unroll
  for (int i = 2; i < 16; i += 2) {
    mx = __builtin_fmaxf(__builtin_fmaxf(mx, p[i >> 2][i & 3]), p[i >> 2][(i & 3) + 1]);
    mn = __builtin_fminf(__builtin_fminf(mn, p[i >> 2][i & 3]), p[i >> 2][(i & 3) + 1]);
  }
  const float L = 1.4426950408889634f;
  const float a = -mx * L, b = mn * L;
  f32x2 e1[8], e2[8];
#pragma unroll
  for (int i = 0; i < 8; i++) {
    const f32x2 v = (i & 1) ? hi2(p[i >> 1]) : lo2(p[i >> 1]);
    const f32x2 x1 = pfma(v, bc(L), bc(a)), x2 = pfma(v, bc(-L), bc(b));
    e1[i] = f32x2{__builtin_amdgcn_exp2f(x1.x), __builtin_amdgcn_exp2f(x1.y)};
    e2[i] = f32x2{__builtin_amdgcn_exp2f(x2.x), __builtin_amdgcn_exp2f(x2.y)};
  }
  const f32x2 s1 = ((e1[0] + e1[1]) + (e1[2] + e1[3])) + ((e1[4] + e1[5]) + (e1[6] + e1[7]));
  const f32x2 s2 = ((e2[0] + e2[1]) + (e2[2] + e2[3])) + ((e2[4] + e2[5]) + (e2[6] + e2[7]));
  const float r1 = __builtin_amdgcn_rcpf(s1.x + s1.y), r2 = __builtin_amdgcn_rcpf(s2.x + s2.y);
#pragma unroll
  for (int i = 0; i < 8; i++) {
    const f32x2 y1 = e1[i] * bc(r1), y2 = e2[i] * bc(r2);
    ph[i] = cvtpk(y1.x, y1.y);
    ph[8 + i] = cvtpk(y2.x, y2.y);
  }
}
DEV u32x4 pack_gran(f32x4 v, u32 t_lo, u32 t_hi) {
  u32x4 o;
  o.x = ((asu(v[0]) + 0x200u) & 0xFFFFFC00u) | t_lo;
  o.y = ((asu(v[1]) + 0x200u) & 0xFFFFFC00u) | t_hi;
  o.z = ((asu(v[2]) + 0x200u) & 0xFFFFFC00u) | t_lo;
  o.w = ((asu(v[3]) + 0x200u) & 0xFFFFFC00u) | t_hi;
  return o;
}
DEV u32 gran_bad(u32x4 v, u32 t_lo, u32 t_hi) { return (v.x ^ t_lo) | (v.y ^ t_hi) | (v.z ^ t_lo) | (v.w ^ t_hi); }
DEV f32x4 gran_val(u32x4 v) { return f32x4{asf(v.x & 0xFFFFFC00u), asf(v.y & 0xFFFFFC00u), asf(v.z & 0xFFFFFC00u), asf(v.w & 0xFFFFFC00u)}; }

DEV void side_b(const Args& a, const u32 lane, const u32 w, const u32 chunk, const u32 pair, const u32 tt, const u32 jc, const u32 k0) {
  TS_DECL
  const u32 g = lane >> 4, c = lane & 15;
  const u32 tg = w & 3, hh = w >> 2;
  const u32 rq = c >> 2, q = c & 3;
  const rsrc_t rX = mkrsrc(a.in[9] + chunk * 131072u, 262144u);
  const rsrc_t rQ = mkrsrc(a.in[10], 1572864u);
  const rsrc_t rP = mkrsrc(a.in[11] + pair * 512u, 1024u);
  const rsrc_t rO = mkrsrc(a.in[12], 524288u);
  const rsrc_t rW = mkrsrc(a.ws, WS_SIZE);
  u32 vx[2], vw[2], vo[2];
#pragma unroll
  for (int i = 0; i < 2; i++) {
    vx[i] = (32 * w + 8 * i + (lane >> 3)) * 1024 + ((lane & 7) ^ ((4 * i + (lane >> 4)) & 7)) * 16;
    const u32 U = 48 * (2 * w + i) + lane, k = (U >> 2) & 63;
    vw[i] = k * 3072 + (U >> 8) * 1024 + pair * 64 + ((U & 3) ^ ((k >> 2) & 2)) * 16;
    vo[i] = (64 * w + 8 * i + (lane >> 3)) * 1024 + jc * 128 + ((lane & 7) ^ (((lane >> 3) & 2) | (i << 2))) * 16;
  }
  asm volatile("s_nop 4");
  if (w == 0) glds(lane * 16, rP, 0, B_PHI);
  auto fetch = [&](int st, int i) {
    if (st < 8) {
      const u32 kb = (st + k0) & 7, d = (st % 3) * B_SLOT;
      if (i < 4) glds(vx[i & 1], rX, kb * 128 + (i >> 1) * 16384, d + w * 4096 + i * 1024);
      else glds48(vw[i - 4], rQ, kb * 196608, d + 32768 + w * 1536 + (i - 4) * 768);
    } else {
      const int n = (st - 8) * 6 + i;
      if (n < 8) glds(vo[n & 1], rO, (n >> 1) * 16384, B_WO + w * 8192 + n * 1024);
    }
  };
#pragma unroll
  for (int st = 0; st < 2; st++)
#pragma unroll
    for (int i = 0; i < 6; i++) fetch(st, i);
  const u32 tag = *(const CST4 u32*)(a.ws + WS_EPB) + 1u;
  asm volatile("" ::"s"(tag));
  TS(2)
  f32x4 qa[4], ka[4], va[4];
#pragma unroll
  for (int t = 0; t < 4; t++) { qa[t] = f32x4{0, 0, 0, 0}; ka[t] = f32x4{0, 0, 0, 0}; va[t] = f32x4{0, 0, 0, 0}; }
  const u32 ax0 = (tg * 64 + c) * 128 + (g ^ (c >> 1)) * 16;
  const u32 aw0 = 32768 + (8 * g + rq) * 64 + ((2 * hh + (q >> 1)) ^ ((g & 1) << 1)) * 16 + (q & 1) * 8;
  u32x4 aphi[4][2];
  u32x4 f0[7], f1[7];
  auto rdf = [&](u32x4* f, int st, int kk) {
    const u32 sb = (st % 3) * B_SLOT, aw = aw0 + sb + kk * 2048;
    f[5] = cat(ldstr(aw + 4096), ldstr(aw + 4096 + 256));
#pragma unroll
    for (int t = 0; t < 4; t++) f[t] = lds128((ax0 ^ (kk * 64)) + sb + t * 2048);
    f[6] = cat(ldstr(aw + 8192), ldstr(aw + 8192 + 256));
    f[4] = cat(ldstr(aw), ldstr(aw + 256));
  };
  auto mm = [&](const u32x4* f, int nst, int i0) {
#pragma unroll
    for (int t = 0; t < 4; t++) { ka[t] = mfma(f[5], f[t], ka[t]); if (t == 1) { SB(); fetch(nst, i0); SB(); } }
#pragma unroll
    for (int t = 0; t < 4; t++) { va[t] = mfma(f[t], f[6], va[t]); if (t == 1) { SB(); fetch(nst, i0 + 1); SB(); } }
#pragma unroll
    for (int t = 0; t < 4; t++) { qa[t] = mfma(f[4], f[t], qa[t]); if (t == 1) { SB(); fetch(nst, i0 + 2); SB(); } }
  };
#pragma unroll
  for (int st = 0; st < 8; st++) {
    SB();
    wait_bar<6>();
    if (st == 0) { TS(3) }
    if (st == 4) { TS(16) }
    if (st == 7) { TS(4) }
    rdf(f0, st, 0);
    SB();
    if (st > 0) mm(f1, st + 2, 0);
    else { fetch(2, 0); fetch(2, 1); fetch(2, 2); }
    SB();
    rdf(f1, st, 1);
    if (st == 0) {
#pragma unroll
      for (int i = 0; i < 4; i++) {
        const u32x2 wd = ldstr(B_PHI + hh * 512 + (4 * g + rq) * 32 + i * 8);
#pragma unroll
        for (int h = 0; h < 2; h++) {
          aphi[i][h].x = rq == 2 * h ? wd.x : 0u; aphi[i][h].y = rq == 2 * h ? wd.y : 0u;
          aphi[i][h].z = rq == 2 * h + 1 ? wd.x : 0u; aphi[i][h].w = rq == 2 * h + 1 ? wd.y : 0u;
          pin(aphi[i][h]);
        }
      }
    }
    SB();
    mm(f0, st + 2, 3);
  }
  SB();
#pragma unroll
  for (int t = 0; t < 4; t++) ka[t] = mfma(f1[5], f1[t], ka[t]);
#pragma unroll
  for (int t = 0; t < 4; t++) va[t] = mfma(f1[t], f1[6], va[t]);
#pragma unroll
  for (int t = 0; t < 4; t++) qa[t] = mfma(f1[4], f1[t], qa[t]);
  SB();
  TS(5)
  const u32 t_lo = tag & 0x3FFu, t_hi = (tag >> 10) & 0x3FFu;
  const f32x4 z4 = f32x4{0, 0, 0, 0};
  {
    const u32x4 b01 = u32x4{cvtpk(ka[0][0], ka[0][1]), cvtpk(ka[0][2], ka[0][3]), cvtpk(ka[1][0], ka[1][1]), cvtpk(ka[1][2], ka[1][3])};
    const u32x4 b23 = u32x4{cvtpk(ka[2][0], ka[2][1]), cvtpk(ka[2][2], ka[2][3]), cvtpk(ka[3][0], ka[3][1]), cvtpk(ka[3][2], ka[3][3])};
    f32x4 p[4];
#pragma unroll
    for (int i = 0; i < 4; i++) p[i] = mfma(aphi[i][0], b01, z4);
#pragma unroll
    for (int i = 0; i < 4; i++) p[i] = mfma(aphi[i][1], b23, p[i]);
    const u32x4 v01 = u32x4{cvtpk(va[0][0], va[0][1]), cvtpk(va[0][2], va[0][3]), cvtpk(va[1][0], va[1][1]), cvtpk(va[1][2], va[1][3])};
    const u32x4 v23 = u32x4{cvtpk(va[2][0], va[2][1]), cvtpk(va[2][2], va[2][3]), cvtpk(va[3][0], va[3][1]), cvtpk(va[3][2], va[3][3])};
    u32 ph[16];
    softmax2(p, ph);
    const u32 wa = w * 4096 + lane * 64, sw = (lane >> 1) & 3;
#pragma unroll
    for (int u = 0; u < 4; u++) sts128(wa + ((u ^ sw) * 16), u32x4{ph[4 * u], ph[4 * u + 1], ph[4 * u + 2], ph[4 * u + 3]});
    const u32 ra = w * 4096 + (4 * g + rq) * 64 + (((q >> 1) ^ ((2 * g + (rq >> 1)) & 3)) * 16) + (q & 1) * 8;
    const u32x4 ones = u32x4{0x3F803F80u, 0x3F803F80u, 0x3F803F80u, 0x3F803F80u};
    f32x4 kv[2], zz[2];
#pragma unroll
    for (int F = 0; F < 2; F++) {
      const u32x4 a01 = cat(ldstr((ra ^ (F * 32))), ldstr((ra ^ (F * 32)) + 1024));
      const u32x4 a23 = cat(ldstr((ra ^ (F * 32)) + 2048), ldstr((ra ^ (F * 32)) + 3072));
      kv[F] = mfma(a01, v01, z4); zz[F] = mfma(a01, ones, z4);
      kv[F] = mfma(a23, v23, kv[F]); zz[F] = mfma(a23, ones, zz[F]);
    }
#pragma unroll
    for (int F = 0; F < 2; F++) {
      sts128(w * 4096 + F * 1024 + lane * 16, __builtin_bit_cast(u32x4, kv[F]));
      if (c == 0) sts128(w * 4096 + 2048 + (F * 4 + g) * 16, __builtin_bit_cast(u32x4, zz[F]));
    }
  }
  lds_bar();
  TS(6)
  if (w < 5) {
    const u32 ra = w < 4 ? (w >> 1) * 16384 + (w & 1) * 1024 + lane * 16 : ((lane >> 3) & 1) * 16384 + 2048 + (lane & 7) * 16;
    const f32x4 sum = (__builtin_bit_cast(f32x4, lds128(ra)) + __builtin_bit_cast(f32x4, lds128(ra + 4096))) +
                      (__builtin_bit_cast(f32x4, lds128(ra + 8192)) + __builtin_bit_cast(f32x4, lds128(ra + 12288)));
    const u32 vo2 = (w < 4 || lane < 16) ? WS_GB + (pair * 8 + chunk) * 4352 + (w * 64 + lane) * 16 : OOB;
    st_sc1(pack_gran(sum, t_lo, t_hi), rW, vo2);
  }
  TS(7)
  u32x4 bq[4];
  {
    const u32x4 b01 = u32x4{cvtpk(qa[0][0], qa[0][1]), cvtpk(qa[0][2], qa[0][3]), cvtpk(qa[1][0], qa[1][1]), cvtpk(qa[1][2], qa[1][3])};
    const u32x4 b23 = u32x4{cvtpk(qa[2][0], qa[2][1]), cvtpk(qa[2][2], qa[2][3]), cvtpk(qa[3][0], qa[3][1]), cvtpk(qa[3][2], qa[3][3])};
    f32x4 p[4];
#pragma unroll
    for (int i = 0; i < 4; i++) p[i] = mfma(aphi[i][0], b01, z4);
#pragma unroll
    for (int i = 0; i < 4; i++) p[i] = mfma(aphi[i][1], b23, p[i]);
    u32 ph[16];
    softmax2(p, ph);
#pragma unroll
    for (int d = 0; d < 4; d++) {
      auto s0 = __builtin_amdgcn_permlane32_swap(ph[d], ph[8 + d], false, false);
      auto s1 = __builtin_amdgcn_permlane32_swap(ph[4 + d], ph[12 + d], false, false);
      auto t0 = __builtin_amdgcn_permlane16_swap(s0[0], s1[0], false, false);
      auto t1 = __builtin_amdgcn_permlane16_swap(s0[1], s1[1], false, false);
      bq[0][d] = t0[0]; bq[1][d] = t0[1]; bq[2][d] = t1[0]; bq[3][d] = t1[1];
    }
    pin(bq[0], bq[1], bq[2], bq[3]);
  }
  TS(8)
  {
    const u32 vg = WS_GB + (pair * 8 + (lane >> 5) * 4) * 4352 + (w * 32 + (lane & 31)) * 16;
    const u32 vz = tg == 0 ? WS_GB + (pair * 8 + (lane >> 3)) * 4352 + (256 + hh * 8 + (lane & 7)) * 16 : OOB;
    u32x4 gr[4], gz;
    const u32 zmask = tg == 0 ? 0x3FFu : 0u;
#pragma unroll 1
    for (int it = 0; it < SPIN_CAP; it++) {
      fence_c();
#pragma unroll
      for (int j = 0; j < 4; j++) gr[j] = ld_sc1(rW, vg, j * 4352);
      gz = ld_sc1(rW, vz, 0);
      const u32 bad = (((gran_bad(gr[0], t_lo, t_hi) | gran_bad(gr[1], t_lo, t_hi)) | (gran_bad(gr[2], t_lo, t_hi) | gran_bad(gr[3], t_lo, t_hi))) & 0x3FFu) |
                      (gran_bad(gz, t_lo, t_hi) & zmask);
      if (none(bad != 0)) break;
    }
    TS(9)
    f32x4 sum = (gran_val(gr[0]) + gran_val(gr[1])) + (gran_val(gr[2]) + gran_val(gr[3]));
#pragma unroll
    for (int i = 0; i < 4; i++) sum[i] = red32(sum[i]);
    if (lane < 32) sts64(B_KVI + hh * 1024 + c * 64 + ((w >> 1) & 1) * 32 + (2 * (w & 1) + g) * 8, u32x2{cvtpk(sum[0], sum[1]), cvtpk(sum[2], sum[3])});
    if (tg == 0) {
      f32x4 z = gran_val(gz);
#pragma unroll
      for (int i = 0; i < 4; i++) { z[i] += asf(dpp<0x128>(asu(z[i]))); z[i] = red32(red16(z[i])); }
      if (lane < 8) sts64(B_ZI + hh * 64 + lane * 8, u32x2{cvtpk(z[0], z[1]), cvtpk(z[2], z[3])});
    }
  }
  lds_bar();
  {
    const u32x4 akv = lds128(B_KVI + hh * 1024 + c * 64 + g * 16);
    const u32x4 az = lds128(B_ZI + hh * 64 + g * 16);
    u32 X[4][2];
#pragma unroll
    for (int t = 0; t < 4; t++) {
      const f32x4 num = mfma(akv, bq[t], z4), den = mfma(az, bq[t], z4);
      const float r = __builtin_amdgcn_rcpf(den[0] + 1e-6f);
      X[t][0] = cvtpk(num[0] * r, num[1] * r); X[t][1] = cvtpk(num[2] * r, num[3] * r);
    }
    const u32 vs = WS_O + ((chunk * 16 + tg * 4 + (g & 1)) * 16 + pair) * 1024 + (2 * hh + (g >> 1)) * 256 + c * 16;
#pragma unroll
    for (int tp = 0; tp < 2; tp++) {
      auto s0 = __builtin_amdgcn_permlane16_swap(X[2 * tp][0], X[2 * tp + 1][0], false, false);
      auto s1 = __builtin_amdgcn_permlane16_swap(X[2 * tp][1], X[2 * tp + 1][1], false, false);
      st_sc1(u32x4{s0[0], s1[0], s0[1], s1[1]}, rW, vs + tp * 32768);
    }
  }
  TS(10)
  wait_bar<0>();
  if (w == 0 && lane < 16) __builtin_amdgcn_raw_buffer_store_b64(u32x2{tag, tag}, rW, WS_FLB + (chunk * 16 + lane) * 128 + pair * 8, 0, 16);
  TS(11)
  const u32 wb = B_WO + hh * 32768 + (8 * g + rq) * 128 + (((q >> 1) ^ ((rq & 2) | ((g & 1) << 2))) * 16) + (q & 1) * 8;
  u32x4 wfr[2][4];
  auto rdw = [&](int ii) {
    const u32 y = ((ii + jc) & 7) * 4096;
#pragma unroll
    for (int ct = 0; ct < 4; ct++) {
      const u32 ad = (wb ^ ((ct ^ (2 * hh)) * 32)) + y;
      wfr[ii & 1][ct] = cat(ldstr(ad), ldstr(ad + 512));
    }
  };
  rdw(0);
  {
    const u32 vf = WS_FLB + (tt * 8 + jc) * 128 + (hh * 8 + (lane & 7)) * 8;
#pragma unroll 1
    for (int it = 0; it < SPIN_CAP; it++) {
      fence_c();
      const u32x2 f = ld64_sc1(rW, vf, 0);
      if (none(((f.x ^ tag) | (f.y ^ tag)) != 0)) break;
    }
  }
  TS(12)
  f32x4 acc[2][4];
#pragma unroll
  for (int i = 0; i < 2; i++)
#pragma unroll
    for (int ct = 0; ct < 4; ct++) acc[i][ct] = z4;
  {
    u32x4 of[8][2];
    const u32 vh = WS_O + ((tt * 8 + 2 * tg) * 16 + hh * 8) * 1024 + lane * 16;
    auto ldo = [&](int ii, int i) { of[ii][i] = ld_sc1(rW, vh, ((ii + jc) & 7) * 1024 + i * 16384); };
#pragma unroll
    for (int ii = 0; ii < DEPB; ii++) { ldo(ii, 0); ldo(ii, 1); }
    SB();
#pragma unroll
    for (int ii = 0; ii < 8; ii++) {
      if (ii < 7) rdw(ii + 1);
      SB();
#pragma unroll
      for (int ct = 0; ct < 4; ct++) {
        acc[0][ct] = mfma(wfr[ii & 1][ct], of[ii][0], acc[0][ct]);
        acc[1][ct] = mfma(wfr[ii & 1][ct], of[ii][1], acc[1][ct]);
        if ((ct == 0 || ct == 2) && ii + DEPB < 8) { SB(); ldo(ii + DEPB, ct >> 1); SB(); }
      }
      SB();
      if (ii == 0) { TS(13) }
    }
  }
  TS(14)
#pragma unroll
  for (int i = 0; i < 2; i++)
#pragma unroll
    for (int e2 = 0; e2 < 2; e2++) sts128(w * 4096 + (i * 2 + e2) * 1024 + lane * 16, __builtin_bit_cast(u32x4, acc[i][2 + e2]));
  lds_bar();
  {
    const rsrc_t rY = mkrsrc(a.out, 8388608u);
    const u32 op = (tt * 128 + tg * 32 + c) * 4096 + (512 + jc * 64 + hh * 32 + 4 * g) * 4;
#pragma unroll
    for (int i = 0; i < 2; i++)
#pragma unroll
      for (int e2 = 0; e2 < 2; e2++) {
        const f32x4 o = acc[i][e2] + __builtin_bit_cast(f32x4, lds128((w ^ 4) * 4096 + (i * 2 + e2) * 1024 + lane * 16));
        const u32 p0 = cvtpk(o[0], o[1]), p1 = cvtpk(o[2], o[3]);
        __builtin_amdgcn_raw_buffer_store_b128(u32x4{p0 << 16, p0 & 0xffff0000u, p1 << 16, p1 & 0xffff0000u}, rY, op + i * 65536 + e2 * 64, 0, OAUX);
      }
  }
  if (tt == 0 && jc == 0 && w == 0 && lane == 0) __builtin_amdgcn_raw_buffer_store_b32(tag, rW, WS_EPB, 0, 16);
  TS(15)
  TS_END
}

__global__ __launch_bounds__(512, 2) void fused_s5_hedgehog(const Args a) {
  __shared__ __attribute__((aligned(1024))) char smem[163840];
  asm volatile("" ::"s"((u32)(size_t)(LDS3 char*)smem) : "memory");
  const u32 b = blockIdx.x, xcd = b & 7, j = b >> 3;
  const u32 lane = threadIdx.x & 63, w = rfl(threadIdx.x >> 6);
  if (xcd < 4) {
    const u32 k0 = ROTA == 0 ? 0u : ROTA == 1 ? 4u * ((j ^ (j >> 3)) & 1) : ((j + 2 * (j >> 3)) & 7);
    side_a(a, lane, w, xcd * 4 + (j >> 3), j & 7, k0);
  } else {
    const u32 x = xcd - 4;
    const u32 k0 = ROTB == 0 ? 0u : ROTB == 1 ? 4u * ((j ^ (j >> 3)) & 1) : ((j + 2 * (j >> 3)) & 7);
    side_b(a, lane, w, (x & 1) * 4 + (j >> 3), (x >> 1) * 8 + (j & 7), x * 4 + (j >> 3), j & 7, k0);
  }
}

__global__ void tiny(char* ws){ if(threadIdx.x==0) ((int*)(ws+WS_TS))[0]=1; }
__global__ void tiny0(char* ws){ }
extern "C" void kernel_launch(void* const* d_in, const int* in_sizes, int n_in, void* d_out, int out_size, void* d_ws,
                              size_t ws_size, hipStream_t stream) {
  Args a;
  for (int i = 0; i < 13; i++) a.in[i] = (const u16*)d_in[i];
  a.out = (float*)d_out;
  a.ws = (char*)d_ws;
  fused_s5_hedgehog<<<256, 512, 0, stream>>>(a);
}
```

```cpp
#include <hip/hip_runtime.h>
#pragma clang diagnostic ignored "-Winline-asm"

typedef unsigned short u16;
typedef unsigned int u32;
typedef unsigned long long u64;
typedef float f32x4 __attribute__((ext_vector_type(4)));
typedef u32 u32x4 __attribute__((ext_vector_type(4)));
typedef u32 u32x2 __attribute__((ext_vector_type(2)));
typedef __bf16 bf16x8 __attribute__((ext_vector_type(8)));
typedef short s16x4 __attribute__((ext_vector_type(4)));
typedef __amdgpu_buffer_rsrc_t rsrc_t;
#define LDS3 __attribute__((address_space(3)))
#define CST4 __attribute__((address_space(4)))
#define DEV __device__ __forceinline__

#define ROTA 1
#define ROTB 1
#define OAUX 16
#define DEPA 5
#define DEPB 5

#define WS_EPA 0u
#define WS_EPB 256u
#define WS_FLA 4096u
#define WS_FLB 20480u
#define WS_GA 65536u
#define WS_GB 196608u
#define WS_H 1048576u
#define WS_O 5242880u
#define WS_SIZE 7340032u
#define WS_TS 8388608u
#define OOB 0xFFFFFF00u
#define SPIN_CAP (1 << 20)

struct Args {
  const u16* in[13];
  float* out;
  char* ws;
};

DEV u32 rfl(u32 x) { return __builtin_amdgcn_readfirstlane(x); }
DEV rsrc_t mkrsrc(const void* p, u32 bytes) { return __builtin_amdgcn_make_buffer_rsrc((void*)p, (short)0, (int)bytes, 0x00020000); }
DEV void glds(u32 voff, rsrc_t srd, u32 soff, u32 ldst) {
  asm volatile("s_mov_b32 m0, %0\n\ts_nop 0\n\tbuffer_load_dwordx4 %1, %2, %3 offen lds" ::"s"(ldst), "v"(voff), "s"(srd), "s"(soff) : "m0", "memory");
}
DEV void glds48(u32 voff, rsrc_t srd, u32 soff, u32 ldst) {
  asm volatile("s_mov_b32 m0, %0\n\ts_mov_b32 exec_hi, 0xffff\n\tbuffer_load_dwordx4 %1, %2, %3 offen lds\n\ts_mov_b32 exec_hi, -1" ::"s"(ldst), "v"(voff), "s"(srd), "s"(soff) : "m0", "memory");
}
template <int N> DEV void wait_bar() { asm volatile("s_waitcnt vmcnt(%0) lgkmcnt(0)\n\ts_barrier" ::"n"(N) : "memory"); }
DEV void lds_bar() { asm volatile("s_waitcnt lgkmcnt(0)\n\ts_barrier" ::: "memory"); }
DEV void raw_bar() { asm volatile("s_barrier" ::: "memory"); }
DEV void fence_c() { asm volatile("" ::: "memory"); }
DEV u32x4 lds128(u32 a) { return *reinterpret_cast<const LDS3 u32x4*>(a); }
DEV u32x2 lds64(u32 a) { return *reinterpret_cast<const LDS3 u32x2*>(a); }
DEV u32 lds16(u32 a) { return *reinterpret_cast<const LDS3 u16*>(a); }
DEV void sts128(u32 a, u32x4 v) { *reinterpret_cast<LDS3 u32x4*>(a) = v; }
DEV void sts64(u32 a, u32x2 v) { *reinterpret_cast<LDS3 u32x2*>(a) = v; }
DEV u32x2 ldstr(u32 a) { return __builtin_bit_cast(u32x2, __builtin_amdgcn_ds_read_tr16_b64_v4i16(reinterpret_cast<LDS3 s16x4*>(a))); }
DEV f32x4 mfma(u32x4 a, u32x4 b, f32x4 c) {
  return __builtin_amdgcn_mfma_f32_16x16x32_bf16(__builtin_bit_cast(bf16x8, a), __builtin_bit_cast(bf16x8, b), c, 0, 0, 0);
}
typedef float f32x2 __attribute__((ext_vector_type(2)));
typedef __bf16 bf16x2 __attribute__((ext_vector_type(2)));
DEV u32 cvtpk(float lo, float hi) { return __builtin_bit_cast(u32, __builtin_convertvector(f32x2{lo, hi}, bf16x2)); }
DEV float asf(u32 x) { return __uint_as_float(x); }
DEV u32 asu(float x) { return __float_as_uint(x); }
DEV float bflo(u32 x) { return asf(x << 16); }
DEV float bfhi(u32 x) { return asf(x & 0xffff0000u); }
DEV u32x4 cat(u32x2 a, u32x2 b) { u32x4 r; r.x = a.x; r.y = a.y; r.z = b.x; r.w = b.y; return r; }
DEV u32x4 ld_sc1(rsrc_t r, u32 voff, u32 soff) { return __builtin_amdgcn_raw_buffer_load_b128(r, voff, soff, 16); }
DEV u32x2 ld64_sc1(rsrc_t r, u32 voff, u32 soff) { return __builtin_amdgcn_raw_buffer_load_b64(r, voff, soff, 16); }
DEV u32x4 ld_pl(rsrc_t r, u32 voff, u32 soff) { return __builtin_amdgcn_raw_buffer_load_b128(r, voff, soff, 0); }
DEV void st_sc1(u32x4 v, rsrc_t r, u32 voff) { __builtin_amdgcn_raw_buffer_store_b128(v, r, voff, 0, 16); }
DEV void st32_sc1(u32 v, rsrc_t r, u32 voff, u32 soff) { __builtin_amdgcn_raw_buffer_store_b32(v, r, voff, soff, 16); }
#define SB() __builtin_amdgcn_sched_barrier(0)
template <class T> DEV void pin(T& x) { asm volatile("" : "+v"(x)); }
template <class T, class... R> DEV void pin(T& x, R&... r) { pin(x); pin(r...); }
DEV f32x2 pfma(f32x2 a, f32x2 b, f32x2 c) { return __builtin_elementwise_fma(a, b, c); }
DEV f32x2 bc(float x) { return f32x2{x, x}; }
DEV f32x2 lo2(f32x4 v) { return __builtin_shufflevector(v, v, 0, 1); }
DEV f32x2 hi2(f32x4 v) { return __builtin_shufflevector(v, v, 2, 3); }
DEV bool none(bool bad) { return __builtin_amdgcn_ballot_w64(bad) == 0; }
DEV float red32(float x) { auto r = __builtin_amdgcn_permlane32_swap(asu(x), asu(x), false, false); return asf(r[0]) + asf(r[1]); }
DEV float red16(float x) { auto r = __builtin_amdgcn_permlane16_swap(asu(x), asu(x), false, false); return asf(r[0]) + asf(r[1]); }
template <int CTRL> DEV u32 dpp(u32 x) { return __builtin_amdgcn_update_dpp(0u, x, CTRL, 0xf, 0xf, true); }
struct cf { float r, i; };
DEV void pinc(cf& x) { asm volatile("" : "+v"(x.r), "+v"(x.i)); }
DEV cf cmul(cf a, cf b) { cf o; o.r = a.r * b.r - a.i * b.i; o.i = a.r * b.i + a.i * b.r; return o; }
DEV cf cfma(cf a, cf b, cf c) {
  cf o; o.r = __builtin_fmaf(a.r, b.r, c.r); o.i = __builtin_fmaf(a.r, b.i, c.i);
  o.r = __builtin_fmaf(-a.i, b.i, o.r); o.i = __builtin_fmaf(a.i, b.r, o.i); return o;
}

#define TS_DECL
#define TS(i)
#define TS_END

#define A_TOP 131072u
#define A_EH (A_TOP + 1024u)

DEV cf csel(u32 g, cf a0, cf a1, cf a2, cf a3) {
  cf o;
  o.r = g == 0 ? a0.r : g == 1 ? a1.r : g == 2 ? a2.r : a3.r;
  o.i = g == 0 ? a0.i : g == 1 ? a1.i : g == 2 ? a2.i : a3.i;
  return o;
}

DEV void side_a(const Args& a, const u32 lane, const u32 w, const u32 chunk, const u32 s, const u32 k0) {
  TS_DECL
  const u32 g = lane >> 4, c = lane & 15;
  const u32 th = w >> 2, cg = w & 3;
  u32 p_lr, p_li, p_dt;
  u32x2 dsk[2];
  {
    const u32 o1 = (s * 64 + cg * 16 + c) * 2, o2 = s * 128 + th * 64 + g * 8;
    asm volatile("global_load_ushort %0, %1, %2" : "=v"(p_lr) : "v"(o1), "s"(a.in[1]) : "memory");
    asm volatile("global_load_ushort %0, %1, %2" : "=v"(p_li) : "v"(o1), "s"(a.in[2]) : "memory");
    asm volatile("global_load_ushort %0, %1, %2" : "=v"(p_dt) : "v"(o1), "s"(a.in[7]) : "memory");
    asm volatile("global_load_dwordx2 %0, %1, %2" : "=v"(dsk[0]) : "v"(o2), "s"(a.in[8]) : "memory");
    asm volatile("global_load_dwordx2 %0, %1, %2 offset:32" : "=v"(dsk[1]) : "v"(o2), "s"(a.in[8]) : "memory");
  }
  const rsrc_t rX = mkrsrc(a.in[0] + chunk * 65536u, 131072u);
  const rsrc_t rB = mkrsrc((w < 4 ? a.in[3] : a.in[4]) + s * 32768u, 65536u);
  const rsrc_t rCr = mkrsrc(a.in[5] + s * 32768u, 65536u);
  const rsrc_t rCi = mkrsrc(a.in[6] + s * 32768u, 65536u);
  const rsrc_t rW = mkrsrc(a.ws, WS_SIZE);
  u32 vx[2], vb[2];
#pragma unroll
  for (int i = 0; i < 2; i++) {
    const u32 rho = 8 * (2 * w + i) + (lane >> 3);
    const u32 unit = (lane & 7) ^ ((rho >> 1) & 7);
    const u32 tok = (rho & 64) + 16 * ((rho >> 2) & 3) + 4 * ((rho >> 4) & 3) + (rho & 3);
    vx[i] = tok * 1024 + unit * 16;
    vb[i] = (rho & 63) * 1024 + unit * 16;
  }
  const u32 vc = (8 * w + (lane >> 3)) * 1024 + (lane & 7) * 16;
  u32x4 cw[4][4];
  auto fetch = [&](int st, int i) {
    if (st < 8) {
      const u32 so = ((st + k0) & 7) * 128, d = (st & 3) * 32768 + w * 2048;
      if (i < 2) glds(vx[i], rX, so, d + i * 1024);
      else glds(vb[i - 2], rB, so, d + 16384 + (i - 2) * 1024);
    } else {
      cw[st - 8][i] = ld_pl((i & 1) ? rCi : rCr, vc, (st - 8) * 256 + (i >> 1) * 128);
    }
  };
#pragma unroll
  for (int st = 0; st < 3; st++)
#pragma unroll
    for (int i = 0; i < 4; i++) fetch(st, i);
  const u32 tag = *(const CST4 u32*)(a.ws + WS_EPA) + 1u;
  asm volatile("" ::"s"(tag));
  TS(2)
  asm volatile("s_waitcnt vmcnt(12)" : "+v"(p_lr), "+v"(p_li), "+v"(p_dt), "+v"(dsk[0]), "+v"(dsk[1])::"memory");
  cf lam, l2, lam16, lam64, c2, wg, wl[4];
  {
    const float L2E = 1.4426950408889634f;
    const float lr = bflo(p_lr), li = bflo(p_li);
    const float dt = bflo(cvtpk(__builtin_amdgcn_exp2f(bflo(p_dt) * L2E), 0.f));
    const float e = __builtin_amdgcn_exp2f(dt * lr * L2E);
    const float rev = __builtin_amdgcn_fractf(dt * li * 0.15915494309189535f);
    lam.r = e * __builtin_amdgcn_cosf(rev); lam.i = e * __builtin_amdgcn_sinf(rev);
    const float nr = lam.r - 1.f, ni = lam.i, rd = 2.f * __builtin_amdgcn_rcpf(lr * lr + li * li);
    c2.r = (nr * lr + ni * li) * rd; c2.i = (ni * lr - nr * li) * rd;
    l2 = cmul(lam, lam);
    const cf l4 = cmul(l2, l2), l8 = cmul(l4, l4);
    lam16 = cmul(l8, l8);
    const cf l32 = cmul(lam16, lam16), l48 = cmul(l32, lam16);
    lam64 = cmul(l32, l32);
    const cf mu = cmul(lam64, lam64), mu2 = cmul(mu, mu), mu3 = cmul(mu2, mu), mu4 = cmul(mu2, mu2);
    wg = csel(g, cf{1.f, 0.f}, lam16, l32, l48);
    wl[0] = csel(g, cf{1.f, 0.f}, mu, mu2, mu3);
    wl[1] = cmul(wl[0], mu4); wl[2] = cmul(wl[1], mu4); wl[3] = cmul(wl[2], mu4);
    pinc(lam); pinc(l2); pinc(lam16); pinc(lam64); pinc(c2); pinc(wg); pinc(wl[0]); pinc(wl[1]); pinc(wl[2]); pinc(wl[3]);
  }
  u32 vo[4], need = 0;
#pragma unroll
  for (int j = 0; j < 4; j++) {
    const int i = (int)chunk - 1 - (int)g - 4 * j;
    vo[j] = i >= 0 ? WS_GA + ((i * 8 + s) * 64 + cg * 16 + c) * 16 : OOB;
    need |= (i >= 0 ? 1u : 0u) << j;
  }
  u32 vga = WS_GA + ((chunk * 8 + s) * 64 + cg * 16 + c) * 16, aeh = A_EH + (cg * 16 + c) * 8;
  u32 vhs = WS_H + ((chunk * 8 + th * 4) * 32 + s * 4 + cg) * 1024 + g * 256 + c * 4;
  pin(vo[0], vo[1], vo[2], vo[3], need, vga, aeh, vhs);
  f32x4 ur[4], ui[4];
#pragma unroll
  for (int t = 0; t < 4; t++) { ur[t] = f32x4{0, 0, 0, 0}; ui[t] = f32x4{0, 0, 0, 0}; }
  const u32 ax0 = (th * 64 + c) * 128 + (g ^ (c >> 1)) * 16;
  const u32 ab0 = 16384 + (cg * 16 + c) * 128 + (g ^ (c >> 1)) * 16;
  const u32 st_skip = (s - k0) & 7;
  const u32 axs = (32 * cg + c) * 128 + ((4 * th + (g >> 1)) ^ (c >> 1)) * 16 + (g & 1) * 8;
  u32x2 xs[2][2];
#pragma unroll
  for (int i = 0; i < 2; i++) { xs[i][0] = u32x2{0, 0}; xs[i][1] = u32x2{0, 0}; }
  u32x4 f0[6], f1[6];
  auto rdf = [&](u32x4* f, int st, int kk) {
    const u32 sb = (st & 3) * 32768;
    f[4] = lds128((ab0 ^ (kk * 64)) + sb);
    f[0] = lds128((ax0 ^ (kk * 64)) + sb);
    f[5] = lds128((ab0 ^ (kk * 64)) + sb + 8192);
#pragma unroll
    for (int t = 1; t < 4; t++) f[t] = lds128((ax0 ^ (kk * 64)) + sb + t * 2048);
  };
  auto mm = [&](const u32x4* f, int nst, int i0) {
#pragma unroll
    for (int t = 0; t < 4; t++) {
      ur[t] = mfma(f[t], f[4], ur[t]);
      ui[t] = mfma(f[t], f[5], ui[t]);
      if (t == 0 || t == 2) { SB(); fetch(nst, i0 + (t >> 1)); SB(); }
    }
  };
#pragma unroll
  for (int st = 0; st < 8; st++) {
    SB();
    wait_bar<8>();
    if (st == 0) { TS(3) }
    if (st == 4) { TS(16) }
    if (st == 7) { TS(4) }
    rdf(f0, st, 0);
    SB();
    if (st > 0) mm(f1, st + 3, 0);
    else { fetch(3, 0); fetch(3, 1); }
    SB();
    rdf(f1, st, 1);
    if (st_skip == (u32)st) {
      const u32 sb = (st & 3) * 32768;
#pragma unroll
      for (int i = 0; i < 2; i++) { xs[i][0] = lds64(axs + sb + i * 2048); fence_c(); xs[i][1] = lds64((axs ^ 32) + sb + i * 2048); fence_c(); }
    }
    SB();
    mm(f0, st + 3, 2);
  }
  SB();
  mm(f1, 11, 0);
  SB();
  fetch(11, 2); fetch(11, 3);
  TS(5)
  cf e;
  {
    f32x2 er = lo2(ur[0]), ei = lo2(ui[0]);
#pragma unroll
    for (int k = 1; k < 8; k++) {
      const f32x2 R = (k & 1) ? hi2(ur[k >> 1]) : lo2(ur[k >> 1]), I = (k & 1) ? hi2(ui[k >> 1]) : lo2(ui[k >> 1]);
      const f32x2 nr = pfma(bc(-l2.i), ei, pfma(bc(l2.r), er, R));
      ei = pfma(bc(l2.i), er, pfma(bc(l2.r), ei, I));
      er = nr;
    }
    e = cfma(lam, cf{er.x, ei.x}, cf{er.y, ei.y});
  }
  cf eg[4];
  {
    auto r = __builtin_amdgcn_permlane32_swap(asu(e.r), asu(e.r), false, false);
    auto q = __builtin_amdgcn_permlane32_swap(asu(e.i), asu(e.i), false, false);
    auto r0 = __builtin_amdgcn_permlane16_swap(r[0], r[0], false, false);
    auto q0 = __builtin_amdgcn_permlane16_swap(q[0], q[0], false, false);
    auto r1 = __builtin_amdgcn_permlane16_swap(r[1], r[1], false, false);
    auto q1 = __builtin_amdgcn_permlane16_swap(q[1], q[1], false, false);
    eg[0].r = asf(r0[0]); eg[1].r = asf(r0[1]); eg[2].r = asf(r1[0]); eg[3].r = asf(r1[1]);
    eg[0].i = asf(q0[0]); eg[1].i = asf(q0[1]); eg[2].i = asf(q1[0]); eg[3].i = asf(q1[1]);
  }
  const cf s2 = cfma(lam16, eg[0], eg[1]), s3 = cfma(lam16, s2, eg[2]), eh = cfma(lam16, s3, eg[3]);
  if (th == 0 && g == 0) sts64(aeh, u32x2{asu(eh.r), asu(eh.i)});
  lds_bar();
  cf pre = csel(g, cf{0.f, 0.f}, eg[0], s2, s3);
  if (th == 1) {
    const u32x2 t = lds64(aeh);
    const cf e0{asf(t.x), asf(t.y)};
    const cf tot = cfma(lam64, e0, eh);
    if (g == 0) st_sc1(u32x4{asu(tot.r), tag, asu(tot.i), tag}, rW, vga);
    pre = cfma(wg, e0, pre);
    wg = cmul(wg, lam64);
  }
  TS(6)
  SB();
  f32x2 hr[8], hi[8], wr[8], wi[8];
  {
    const cf p2 = cmul(c2, pre);
    wg = cmul(c2, wg);
    cf h{p2.r, -p2.i};
    const cf lc{lam.r, -lam.i};
#pragma unroll
    for (int k = 0; k < 8; k++) {
      const f32x2 R = (k & 1) ? hi2(ur[k >> 1]) : lo2(ur[k >> 1]), I = (k & 1) ? hi2(ui[k >> 1]) : lo2(ui[k >> 1]);
      const f32x2 vr = pfma(bc(-c2.i), I, bc(c2.r) * R), vi = pfma(bc(-c2.i), R, bc(-c2.r) * I);
      const cf h0 = cfma(lc, h, cf{vr.x, vi.x});
      h = cfma(lc, h0, cf{vr.y, vi.y});
      hr[k] = f32x2{h0.r, h.r}; hi[k] = f32x2{h0.i, h.i};
    }
    wr[0] = f32x2{lam.r, l2.r}; wi[0] = f32x2{-lam.i, -l2.i};
#pragma unroll
    for (int k = 1; k < 8; k++) {
      wr[k] = pfma(bc(l2.i), wi[k - 1], bc(l2.r) * wr[k - 1]);
      wi[k] = pfma(bc(-l2.i), wr[k - 1], bc(l2.r) * wi[k - 1]);
    }
#pragma unroll
    for (int k = 0; k < 8; k++) pin(hr[k], hi[k], wr[k], wi[k]);
    pinc(wg);
  }
  SB();
  u32x4 gr[4];
#pragma unroll
  for (int j = 0; j < 4; j++) gr[j] = ld_sc1(rW, vo[j], 0);
  SB();
  TS(17)
  f32x4 acc[2][4];
#pragma unroll
  for (int i = 0; i < 2; i++) {
#pragma unroll
    for (int e2 = 0; e2 < 2; e2++) {
      const u32 p0 = cvtpk(bflo(xs[i][e2].x) * bflo(dsk[e2].x), bfhi(xs[i][e2].x) * bfhi(dsk[e2].x));
      const u32 p1 = cvtpk(bflo(xs[i][e2].y) * bflo(dsk[e2].y), bfhi(xs[i][e2].y) * bfhi(dsk[e2].y));
      acc[i][e2] = f32x4{bflo(p0), bfhi(p0), bflo(p1), bfhi(p1)};
      acc[i][2 + e2] = f32x4{0, 0, 0, 0};
      pin(acc[i][e2]);
    }
  }
  {
    const u32 u = lane & 7, pb = ((u >> 1) - 2 * s) & 15;
    const u32 rest = (w >> 1) * 1024 + (u & 1) * 512 + ((8 * (w & 1) + (lane >> 3)) ^ (2 * (pb & 3) + (u & 1))) * 16;
#pragma unroll
    for (int p = 0; p < 4; p++)
#pragma unroll
      for (int hf = 0; hf < 2; hf++) {
        const u32 ad = ((((pb + (p & 1) * 8 + hf * 4) & 15) << 12) | (rest ^ ((p >> 1) * 2048))) + (p >> 1) * 65536;
        const u32x4 R = cw[p][2 * hf], I = cw[p][2 * hf + 1];
        sts128(ad, u32x4{__builtin_amdgcn_perm(I.x, R.x, 0x05040100u), __builtin_amdgcn_perm(I.x, R.x, 0x07060302u),
                         __builtin_amdgcn_perm(I.y, R.y, 0x05040100u), __builtin_amdgcn_perm(I.y, R.y, 0x07060302u)});
        sts128(ad + 256, u32x4{__builtin_amdgcn_perm(I.z, R.z, 0x05040100u), __builtin_amdgcn_perm(I.z, R.z, 0x07060302u),
                               __builtin_amdgcn_perm(I.w, R.w, 0x05040100u), __builtin_amdgcn_perm(I.w, R.w, 0x07060302u)});
      }
  }
  SB();
  TS(18)
  {
#pragma unroll 1
    for (int it = 0; it < SPIN_CAP; it++) {
      u32 have = 0;
#pragma unroll
      for (int j = 0; j < 4; j++) have |= (((gr[j].y ^ tag) | (gr[j].w ^ tag)) == 0 ? 1u : 0u) << j;
      if (none((need & ~have) != 0)) break;
      fence_c();
#pragma unroll
      for (int j = 0; j < 4; j++) gr[j] = ld_sc1(rW, vo[j], 0);
    }
  }
  TS(7)
  {
    cf H = cmul(wl[0], cf{asf(gr[0].x), asf(gr[0].z)});
#pragma unroll
    for (int j = 1; j < 4; j++) H = cfma(wl[j], cf{asf(gr[j].x), asf(gr[j].z)}, H);
    H.r = red16(red32(H.r)); H.i = red16(red32(H.i));
    const cf z = cmul(wg, H);
#pragma unroll
    for (int k = 0; k < 8; k++) {
      const f32x2 yr = pfma(wi[k], bc(z.i), pfma(wr[k], bc(z.r), hr[k]));
      const f32x2 yi = pfma(wr[k], bc(-z.i), pfma(wi[k], bc(z.r), hi[k]));
      st32_sc1(cvtpk(yr.x, yi.x), rW, vhs + (k & 1) * 128, (k >> 1) * 32768);
      st32_sc1(cvtpk(yr.y, yi.y), rW, vhs + (k & 1) * 128 + 64, (k >> 1) * 32768);
    }
  }
  TS(8)
  wait_bar<0>();
  if (w == 0 && lane < 8) __builtin_amdgcn_raw_buffer_store_b64(u32x2{tag, tag}, rW, WS_FLA + (chunk * 8 + lane) * 128 + s * 8, 0, 16);
  TS(9)
  u32 cb[4];
#pragma unroll
  for (int i = 0; i < 4; i++) cb[i] = th * 65536 + g * 256 + ((c ^ (2 * i + (g >> 1))) * 16);
  u32x4 cfr[2][4];
  auto rdc = [&](int ii) {
#pragma unroll
    for (int ct = 0; ct < 4; ct++) cfr[ii & 1][ct] = lds128(cb[ii & 3] + ii * 4096 + ct * 1024);
  };
  rdc(0);
  {
    const u32 vf = WS_FLA + (chunk * 8 + s) * 128 + (th * 4 + (lane & 3)) * 8;
#pragma unroll 1
    for (int it = 0; it < SPIN_CAP; it++) {
      fence_c();
      const u32x2 f = ld64_sc1(rW, vf, 0);
      if (none(((f.x ^ tag) | (f.y ^ tag)) != 0)) break;
    }
  }
  TS(10)
  {
    u32x4 hf[16][2];
    const u32 vh = WS_H + ((chunk * 8 + 2 * cg) * 32 + th * 16) * 1024 + c * 64 + g * 16;
    auto ldh = [&](int ii, int i) { hf[ii][i] = ld_sc1(rW, vh, ((ii + 2 * s) & 15) * 1024 + i * 32768); };
#pragma unroll
    for (int ii = 0; ii < DEPA; ii++) { ldh(ii, 0); ldh(ii, 1); }
    SB();
    TS(11)
#pragma unroll
    for (int ii = 0; ii < 16; ii++) {
      if (ii < 15) rdc(ii + 1);
      SB();
#pragma unroll
      for (int ct = 0; ct < 4; ct++) {
        acc[0][ct] = mfma(cfr[ii & 1][ct], hf[ii][0], acc[0][ct]);
        acc[1][ct] = mfma(cfr[ii & 1][ct], hf[ii][1], acc[1][ct]);
        if ((ct == 0 || ct == 2) && ii + DEPA < 16) { SB(); ldh(ii + DEPA, ct >> 1); SB(); }
      }
      SB();
      if (ii == 0) { TS(12) }
    }
  }
  TS(13)
#pragma unroll
  for (int i = 0; i < 2; i++)
#pragma unroll
    for (int e2 = 0; e2 < 2; e2++) sts128(A_TOP + w * 4096 + (i * 2 + e2) * 1024 + lane * 16, __builtin_bit_cast(u32x4, acc[i][2 + e2]));
  lds_bar();
  TS(19)
  {
    const rsrc_t rY = mkrsrc(a.out, 8388608u);
    const u32 op = (chunk * 128 + (cg >> 1) * 64 + 16 * (c >> 2) + 8 * (cg & 1) + (c & 3)) * 4096 + (s * 64 + th * 32 + 4 * g) * 4;
#pragma unroll
    for (int i = 0; i < 2; i++)
#pragma unroll
      for (int e2 = 0; e2 < 2; e2++) {
        const f32x4 o = acc[i][e2] + __builtin_bit_cast(f32x4, lds128(A_TOP + (w ^ 4) * 4096 + (i * 2 + e2) * 1024 + lane * 16));
        __builtin_amdgcn_raw_buffer_store_b128(__builtin_bit_cast(u32x4, o), rY, op + i * 16384 + e2 * 64, 0, OAUX);
      }
  }
  if (chunk == 15 && s == 0 && w == 0 && lane == 0) __builtin_amdgcn_raw_buffer_store_b32(tag, rW, WS_EPA, 0, 16);
  TS(14)
  TS_END
}

#define B_SLOT 45056u
#define B_WO 90112u
#define B_PHI 155648u
#define B_KVI (B_PHI + 1024u)
#define B_ZI (B_PHI + 3072u)

DEV void softmax2(const f32x4* p, u32* ph) {
  float mx = __builtin_fmaxf(p[0][0], p[0][1]), mn = __builtin_fminf(p[0][0], p[0][1]);
#pragma unroll
  for (int i = 2; i < 16; i += 2) {
    mx = __builtin_fmaxf(__builtin_fmaxf(mx, p[i >> 2][i & 3]), p[i >> 2][(i & 3) + 1]);
    mn = __builtin_fminf(__builtin_fminf(mn, p[i >> 2][i & 3]), p[i >> 2][(i & 3) + 1]);
  }
  const float L = 1.4426950408889634f;
  const float a = -mx * L, b = mn * L;
  f32x2 e1[8], e2[8];
#pragma unroll
  for (int i = 0; i < 8; i++) {
    const f32x2 v = (i & 1) ? hi2(p[i >> 1]) : lo2(p[i >> 1]);
    const f32x2 x1 = pfma(v, bc(L), bc(a)), x2 = pfma(v, bc(-L), bc(b));
    e1[i] = f32x2{__builtin_amdgcn_exp2f(x1.x), __builtin_amdgcn_exp2f(x1.y)};
    e2[i] = f32x2{__builtin_amdgcn_exp2f(x2.x), __builtin_amdgcn_exp2f(x2.y)};
  }
  const f32x2 s1 = ((e1[0] + e1[1]) + (e1[2] + e1[3])) + ((e1[4] + e1[5]) + (e1[6] + e1[7]));
  const f32x2 s2 = ((e2[0] + e2[1]) + (e2[2] + e2[3])) + ((e2[4] + e2[5]) + (e2[6] + e2[7]));
  const float r1 = __builtin_amdgcn_rcpf(s1.x + s1.y), r2 = __builtin_amdgcn_rcpf(s2.x + s2.y);
#pragma unroll
  for (int i = 0; i < 8; i++) {
    const f32x2 y1 = e1[i] * bc(r1), y2 = e2[i] * bc(r2);
    ph[i] = cvtpk(y1.x, y1.y);
    ph[8 + i] = cvtpk(y2.x, y2.y);
  }
}
DEV u32x4 pack_gran(f32x4 v, u32 t_lo, u32 t_hi) {
  u32x4 o;
  o.x = ((asu(v[0]) + 0x200u) & 0xFFFFFC00u) | t_lo;
  o.y = ((asu(v[1]) + 0x200u) & 0xFFFFFC00u) | t_hi;
  o.z = ((asu(v[2]) + 0x200u) & 0xFFFFFC00u) | t_lo;
  o.w = ((asu(v[3]) + 0x200u) & 0xFFFFFC00u) | t_hi;
  return o;
}
DEV u32 gran_bad(u32x4 v, u32 t_lo, u32 t_hi) { return (v.x ^ t_lo) | (v.y ^ t_hi) | (v.z ^ t_lo) | (v.w ^ t_hi); }
DEV f32x4 gran_val(u32x4 v) { return f32x4{asf(v.x & 0xFFFFFC00u), asf(v.y & 0xFFFFFC00u), asf(v.z & 0xFFFFFC00u), asf(v.w & 0xFFFFFC00u)}; }

DEV void side_b(const Args& a, const u32 lane, const u32 w, const u32 chunk, const u32 pair, const u32 tt, const u32 jc, const u32 k0) {
  TS_DECL
  const u32 g = lane >> 4, c = lane & 15;
  const u32 tg = w & 3, hh = w >> 2;
  const u32 rq = c >> 2, q = c & 3;
  const rsrc_t rX = mkrsrc(a.in[9] + chunk * 131072u, 262144u);
  const rsrc_t rQ = mkrsrc(a.in[10], 1572864u);
  const rsrc_t rP = mkrsrc(a.in[11] + pair * 512u, 1024u);
  const rsrc_t rO = mkrsrc(a.in[12], 524288u);
  const rsrc_t rW = mkrsrc(a.ws, WS_SIZE);
  u32 vx[2], vw[2], vo[2];
#pragma unroll
  for (int i = 0; i < 2; i++) {
    vx[i] = (32 * w + 8 * i + (lane >> 3)) * 1024 + ((lane & 7) ^ ((4 * i + (lane >> 4)) & 7)) * 16;
    const u32 U = 48 * (2 * w + i) + lane, k = (U >> 2) & 63;
    vw[i] = k * 3072 + (U >> 8) * 1024 + pair * 64 + ((U & 3) ^ ((k >> 2) & 2)) * 16;
    vo[i] = (64 * w + 8 * i + (lane >> 3)) * 1024 + jc * 128 + ((lane & 7) ^ (((lane >> 3) & 2) | (i << 2))) * 16;
  }
  asm volatile("s_nop 4");
  if (w == 0) glds(lane * 16, rP, 0, B_PHI);
  auto fetch = [&](int st, int i) {
    if (st < 8) {
      const u32 kb = (st + k0) & 7, d = (st % 3) * B_SLOT;
      if (i < 4) glds(vx[i & 1], rX, kb * 128 + (i >> 1) * 16384, d + w * 4096 + i * 1024);
      else glds48(vw[i - 4], rQ, kb * 196608, d + 32768 + w * 1536 + (i - 4) * 768);
    } else {
      const int n = (st - 8) * 6 + i;
      if (n < 8) glds(vo[n & 1], rO, (n >> 1) * 16384, B_WO + w * 8192 + n * 1024);
    }
  };
#pragma unroll
  for (int st = 0; st < 2; st++)
#pragma unroll
    for (int i = 0; i < 6; i++) fetch(st, i);
  const u32 tag = *(const CST4 u32*)(a.ws + WS_EPB) + 1u;
  asm volatile("" ::"s"(tag));
  TS(2)
  f32x4 qa[4], ka[4], va[4];
#pragma unroll
  for (int t = 0; t < 4; t++) { qa[t] = f32x4{0, 0, 0, 0}; ka[t] = f32x4{0, 0, 0, 0}; va[t] = f32x4{0, 0, 0, 0}; }
  const u32 ax0 = (tg * 64 + c) * 128 + (g ^ (c >> 1)) * 16;
  const u32 aw0 = 32768 + (8 * g + rq) * 64 + ((2 * hh + (q >> 1)) ^ ((g & 1) << 1)) * 16 + (q & 1) * 8;
  u32x4 aphi[4][2];
  u32x4 f0[7], f1[7];
  auto rdf = [&](u32x4* f, int st, int kk) {
    const u32 sb = (st % 3) * B_SLOT, aw = aw0 + sb + kk * 2048;
    f[5] = cat(ldstr(aw + 4096), ldstr(aw + 4096 + 256));
#pragma unroll
    for (int t = 0; t < 4; t++) f[t] = lds128((ax0 ^ (kk * 64)) + sb + t * 2048);
    f[6] = cat(ldstr(aw + 8192), ldstr(aw + 8192 + 256));
    f[4] = cat(ldstr(aw), ldstr(aw + 256));
  };
  auto mm = [&](const u32x4* f, int nst, int i0) {
#pragma unroll
    for (int t = 0; t < 4; t++) { ka[t] = mfma(f[5], f[t], ka[t]); if (t == 1) { SB(); fetch(nst, i0); SB(); } }
#pragma unroll
    for (int t = 0; t < 4; t++) { va[t] = mfma(f[t], f[6], va[t]); if (t == 1) { SB(); fetch(nst, i0 + 1); SB(); } }
#pragma unroll
    for (int t = 0; t < 4; t++) { qa[t] = mfma(f[4], f[t], qa[t]); if (t == 1) { SB(); fetch(nst, i0 + 2); SB(); } }
  };
#pragma unroll
  for (int st = 0; st < 8; st++) {
    SB();
    wait_bar<6>();
    if (st == 0) { TS(3) }
    if (st == 4) { TS(16) }
    if (st == 7) { TS(4) }
    rdf(f0, st, 0);
    SB();
    if (st > 0) mm(f1, st + 2, 0);
    else { fetch(2, 0); fetch(2, 1); fetch(2, 2); }
    SB();
    rdf(f1, st, 1);
    if (st == 0) {
#pragma unroll
      for (int i = 0; i < 4; i++) {
        const u32x2 wd = ldstr(B_PHI + hh * 512 + (4 * g + rq) * 32 + i * 8);
#pragma unroll
        for (int h = 0; h < 2; h++) {
          aphi[i][h].x = rq == 2 * h ? wd.x : 0u; aphi[i][h].y = rq == 2 * h ? wd.y : 0u;
          aphi[i][h].z = rq == 2 * h + 1 ? wd.x : 0u; aphi[i][h].w = rq == 2 * h + 1 ? wd.y : 0u;
          pin(aphi[i][h]);
        }
      }
    }
    SB();
    mm(f0, st + 2, 3);
  }
  SB();
#pragma unroll
  for (int t = 0; t < 4; t++) ka[t] = mfma(f1[5], f1[t], ka[t]);
#pragma unroll
  for (int t = 0; t < 4; t++) va[t] = mfma(f1[t], f1[6], va[t]);
#pragma unroll
  for (int t = 0; t < 4; t++) qa[t] = mfma(f1[4], f1[t], qa[t]);
  SB();
  TS(5)
  const u32 t_lo = tag & 0x3FFu, t_hi = (tag >> 10) & 0x3FFu;
  const f32x4 z4 = f32x4{0, 0, 0, 0};
  {
    const u32x4 b01 = u32x4{cvtpk(ka[0][0], ka[0][1]), cvtpk(ka[0][2], ka[0][3]), cvtpk(ka[1][0], ka[1][1]), cvtpk(ka[1][2], ka[1][3])};
    const u32x4 b23 = u32x4{cvtpk(ka[2][0], ka[2][1]), cvtpk(ka[2][2], ka[2][3]), cvtpk(ka[3][0], ka[3][1]), cvtpk(ka[3][2], ka[3][3])};
    f32x4 p[4];
#pragma unroll
    for (int i = 0; i < 4; i++) p[i] = mfma(aphi[i][0], b01, z4);
#pragma unroll
    for (int i = 0; i < 4; i++) p[i] = mfma(aphi[i][1], b23, p[i]);
    const u32x4 v01 = u32x4{cvtpk(va[0][0], va[0][1]), cvtpk(va[0][2], va[0][3]), cvtpk(va[1][0], va[1][1]), cvtpk(va[1][2], va[1][3])};
    const u32x4 v23 = u32x4{cvtpk(va[2][0], va[2][1]), cvtpk(va[2][2], va[2][3]), cvtpk(va[3][0], va[3][1]), cvtpk(va[3][2], va[3][3])};
    u32 ph[16];
    softmax2(p, ph);
    const u32 wa = w * 4096 + lane * 64, sw = (lane >> 1) & 3;
#pragma unroll
    for (int u = 0; u < 4; u++) sts128(wa + ((u ^ sw) * 16), u32x4{ph[4 * u], ph[4 * u + 1], ph[4 * u + 2], ph[4 * u + 3]});
    const u32 ra = w * 4096 + (4 * g + rq) * 64 + (((q >> 1) ^ ((2 * g + (rq >> 1)) & 3)) * 16) + (q & 1) * 8;
    const u32x4 ones = u32x4{0x3F803F80u, 0x3F803F80u, 0x3F803F80u, 0x3F803F80u};
    f32x4 kv[2], zz[2];
#pragma unroll
    for (int F = 0; F < 2; F++) {
      const u32x4 a01 = cat(ldstr((ra ^ (F * 32))), ldstr((ra ^ (F * 32)) + 1024));
      const u32x4 a23 = cat(ldstr((ra ^ (F * 32)) + 2048), ldstr((ra ^ (F * 32)) + 3072));
      kv[F] = mfma(a01, v01, z4); zz[F] = mfma(a01, ones, z4);
      kv[F] = mfma(a23, v23, kv[F]); zz[F] = mfma(a23, ones, zz[F]);
    }
#pragma unroll
    for (int F = 0; F < 2; F++) {
      sts128(w * 4096 + F * 1024 + lane * 16, __builtin_bit_cast(u32x4, kv[F]));
      if (c == 0) sts128(w * 4096 + 2048 + (F * 4 + g) * 16, __builtin_bit_cast(u32x4, zz[F]));
    }
  }
  lds_bar();
  TS(6)
  if (w < 5) {
    const u32 ra = w < 4 ? (w >> 1) * 16384 + (w & 1) * 1024 + lane * 16 : ((lane >> 3) & 1) * 16384 + 2048 + (lane & 7) * 16;
    const f32x4 sum = (__builtin_bit_cast(f32x4, lds128(ra)) + __builtin_bit_cast(f32x4, lds128(ra + 4096))) +
                      (__builtin_bit_cast(f32x4, lds128(ra + 8192)) + __builtin_bit_cast(f32x4, lds128(ra + 12288)));
    const u32 vo2 = (w < 4 || lane < 16) ? WS_GB + (pair * 8 + chunk) * 4352 + (w * 64 + lane) * 16 : OOB;
    st_sc1(pack_gran(sum, t_lo, t_hi), rW, vo2);
  }
  TS(7)
  u32x4 bq[4];
  {
    const u32x4 b01 = u32x4{cvtpk(qa[0][0], qa[0][1]), cvtpk(qa[0][2], qa[0][3]), cvtpk(qa[1][0], qa[1][1]), cvtpk(qa[1][2], qa[1][3])};
    const u32x4 b23 = u32x4{cvtpk(qa[2][0], qa[2][1]), cvtpk(qa[2][2], qa[2][3]), cvtpk(qa[3][0], qa[3][1]), cvtpk(qa[3][2], qa[3][3])};
    f32x4 p[4];
#pragma unroll
    for (int i = 0; i < 4; i++) p[i] = mfma(aphi[i][0], b01, z4);
#pragma unroll
    for (int i = 0; i < 4; i++) p[i] = mfma(aphi[i][1], b23, p[i]);
    u32 ph[16];
    softmax2(p, ph);
#pragma unroll
    for (int d = 0; d < 4; d++) {
      auto s0 = __builtin_amdgcn_permlane32_swap(ph[d], ph[8 + d], false, false);
      auto s1 = __builtin_amdgcn_permlane32_swap(ph[4 + d], ph[12 + d], false, false);
      auto t0 = __builtin_amdgcn_permlane16_swap(s0[0], s1[0], false, false);
      auto t1 = __builtin_amdgcn_permlane16_swap(s0[1], s1[1], false, false);
      bq[0][d] = t0[0]; bq[1][d] = t0[1]; bq[2][d] = t1[0]; bq[3][d] = t1[1];
    }
    pin(bq[0], bq[1], bq[2], bq[3]);
  }
  TS(8)
  {
    const u32 vg = WS_GB + (pair * 8 + (lane >> 5) * 4) * 4352 + (w * 32 + (lane & 31)) * 16;
    const u32 vz = tg == 0 ? WS_GB + (pair * 8 + (lane >> 3)) * 4352 + (256 + hh * 8 + (lane & 7)) * 16 : OOB;
    u32x4 gr[4], gz;
    const u32 zmask = tg == 0 ? 0x3FFu : 0u;
#pragma unroll 1
    for (int it = 0; it < SPIN_CAP; it++) {
      fence_c();
#pragma unroll
      for (int j = 0; j < 4; j++) gr[j] = ld_sc1(rW, vg, j * 4352);
      gz = ld_sc1(rW, vz, 0);
      const u32 bad = (((gran_bad(gr[0], t_lo, t_hi) | gran_bad(gr[1], t_lo, t_hi)) | (gran_bad(gr[2], t_lo, t_hi) | gran_bad(gr[3], t_lo, t_hi))) & 0x3FFu) |
                      (gran_bad(gz, t_lo, t_hi) & zmask);
      if (none(bad != 0)) break;
    }
    TS(9)
    f32x4 sum = (gran_val(gr[0]) + gran_val(gr[1])) + (gran_val(gr[2]) + gran_val(gr[3]));
#pragma unroll
    for (int i = 0; i < 4; i++) sum[i] = red32(sum[i]);
    if (lane < 32) sts64(B_KVI + hh * 1024 + c * 64 + ((w >> 1) & 1) * 32 + (2 * (w & 1) + g) * 8, u32x2{cvtpk(sum[0], sum[1]), cvtpk(sum[2], sum[3])});
    if (tg == 0) {
      f32x4 z = gran_val(gz);
#pragma unroll
      for (int i = 0; i < 4; i++) { z[i] += asf(dpp<0x128>(asu(z[i]))); z[i] = red32(red16(z[i])); }
      if (lane < 8) sts64(B_ZI + hh * 64 + lane * 8, u32x2{cvtpk(z[0], z[1]), cvtpk(z[2], z[3])});
    }
  }
  lds_bar();
  {
    const u32x4 akv = lds128(B_KVI + hh * 1024 + c * 64 + g * 16);
    const u32x4 az = lds128(B_ZI + hh * 64 + g * 16);
    u32 X[4][2];
#pragma unroll
    for (int t = 0; t < 4; t++) {
      const f32x4 num = mfma(akv, bq[t], z4), den = mfma(az, bq[t], z4);
      const float r = __builtin_amdgcn_rcpf(den[0] + 1e-6f);
      X[t][0] = cvtpk(num[0] * r, num[1] * r); X[t][1] = cvtpk(num[2] * r, num[3] * r);
    }
    const u32 vs = WS_O + ((chunk * 16 + tg * 4 + (g & 1)) * 16 + pair) * 1024 + (2 * hh + (g >> 1)) * 256 + c * 16;
#pragma unroll
    for (int tp = 0; tp < 2; tp++) {
      auto s0 = __builtin_amdgcn_permlane16_swap(X[2 * tp][0], X[2 * tp + 1][0], false, false);
      auto s1 = __builtin_amdgcn_permlane16_swap(X[2 * tp][1], X[2 * tp + 1][1], false, false);
      st_sc1(u32x4{s0[0], s1[0], s0[1], s1[1]}, rW, vs + tp * 32768);
    }
  }
  TS(10)
  wait_bar<0>();
  if (w == 0 && lane < 16) __builtin_amdgcn_raw_buffer_store_b64(u32x2{tag, tag}, rW, WS_FLB + (chunk * 16 + lane) * 128 + pair * 8, 0, 16);
  TS(11)
  const u32 wb = B_WO + hh * 32768 + (8 * g + rq) * 128 + (((q >> 1) ^ ((rq & 2) | ((g & 1) << 2))) * 16) + (q & 1) * 8;
  u32x4 wfr[2][4];
  auto rdw = [&](int ii) {
    const u32 y = ((ii + jc) & 7) * 4096;
#pragma unroll
    for (int ct = 0; ct < 4; ct++) {
      const u32 ad = (wb ^ ((ct ^ (2 * hh)) * 32)) + y;
      wfr[ii & 1][ct] = cat(ldstr(ad), ldstr(ad + 512));
    }
  };
  rdw(0);
  {
    const u32 vf = WS_FLB + (tt * 8 + jc) * 128 + (hh * 8 + (lane & 7)) * 8;
#pragma unroll 1
    for (int it = 0; it < SPIN_CAP; it++) {
      fence_c();
      const u32x2 f = ld64_sc1(rW, vf, 0);
      if (none(((f.x ^ tag) | (f.y ^ tag)) != 0)) break;
    }
  }
  TS(12)
  f32x4 acc[2][4];
#pragma unroll
  for (int i = 0; i < 2; i++)
#pragma unroll
    for (int ct = 0; ct < 4; ct++) acc[i][ct] = z4;
  {
    u32x4 of[8][2];
    const u32 vh = WS_O + ((tt * 8 + 2 * tg) * 16 + hh * 8) * 1024 + lane * 16;
    auto ldo = [&](int ii, int i) { of[ii][i] = ld_sc1(rW, vh, ((ii + jc) & 7) * 1024 + i * 16384); };
#pragma unroll
    for (int ii = 0; ii < DEPB; ii++) { ldo(ii, 0); ldo(ii, 1); }
    SB();
#pragma unroll
    for (int ii = 0; ii < 8; ii++) {
      if (ii < 7) rdw(ii + 1);
      SB();
#pragma unroll
      for (int ct = 0; ct < 4; ct++) {
        acc[0][ct] = mfma(wfr[ii & 1][ct], of[ii][0], acc[0][ct]);
        acc[1][ct] = mfma(wfr[ii & 1][ct], of[ii][1], acc[1][ct]);
        if ((ct == 0 || ct == 2) && ii + DEPB < 8) { SB(); ldo(ii + DEPB, ct >> 1); SB(); }
      }
      SB();
      if (ii == 0) { TS(13) }
    }
  }
  TS(14)
#pragma unroll
  for (int i = 0; i < 2; i++)
#pragma unroll
    for (int e2 = 0; e2 < 2; e2++) sts128(w * 4096 + (i * 2 + e2) * 1024 + lane * 16, __builtin_bit_cast(u32x4, acc[i][2 + e2]));
  lds_bar();
  {
    const rsrc_t rY = mkrsrc(a.out, 8388608u);
    const u32 op = (tt * 128 + tg * 32 + c) * 4096 + (512 + jc * 64 + hh * 32 + 4 * g) * 4;
#pragma unroll
    for (int i = 0; i < 2; i++)
#pragma unroll
      for (int e2 = 0; e2 < 2; e2++) {
        const f32x4 o = acc[i][e2] + __builtin_bit_cast(f32x4, lds128((w ^ 4) * 4096 + (i * 2 + e2) * 1024 + lane * 16));
        const u32 p0 = cvtpk(o[0], o[1]), p1 = cvtpk(o[2], o[3]);
        __builtin_amdgcn_raw_buffer_store_b128(u32x4{p0 << 16, p0 & 0xffff0000u, p1 << 16, p1 & 0xffff0000u}, rY, op + i * 65536 + e2 * 64, 0, OAUX);
      }
  }
  if (tt == 0 && jc == 0 && w == 0 && lane == 0) __builtin_amdgcn_raw_buffer_store_b32(tag, rW, WS_EPB, 0, 16);
  TS(15)
  TS_END
}

__global__ __launch_bounds__(512, 2) void fused_s5_hedgehog(const Args a) {
  __shared__ __attribute__((aligned(1024))) char smem[163840];
  asm volatile("" ::"s"((u32)(size_t)(LDS3 char*)smem) : "memory");
  const u32 b = blockIdx.x, xcd = b & 7, j = b >> 3;
  const u32 lane = threadIdx.x & 63, w = rfl(threadIdx.x >> 6);
  if (xcd < 4) {
    const u32 k0 = ROTA == 0 ? 0u : ROTA == 1 ? 4u * ((j ^ (j >> 3)) & 1) : ((j + 2 * (j >> 3)) & 7);
    side_a(a, lane, w, xcd * 4 + (j >> 3), j & 7, k0);
  } else {
    const u32 x = xcd - 4;
    const u32 k0 = ROTB == 0 ? 0u : ROTB == 1 ? 4u * ((j ^ (j >> 3)) & 1) : ((j + 2 * (j >> 3)) & 7);
    side_b(a, lane, w, (x & 1) * 4 + (j >> 3), (x >> 1) * 8 + (j & 7), x * 4 + (j >> 3), j & 7, k0);
  }
}

__device__ __forceinline__ u64 rt2_() { u64 t; asm volatile("s_memrealtime %0\n\ts_waitcnt lgkmcnt(0)" : "=s"(t)::"memory"); return t; }
__global__ void slp(char* ws){ u64 t0=rt2_(); while(rt2_()-t0<5000) __builtin_amdgcn_s_sleep(2); }
template<int AUX> __global__ __launch_bounds__(512) void wr(char* ws){ const rsrc_t r=mkrsrc(ws+(16u<<20),8u<<20); for(int i=0;i<4;i++) __builtin_amdgcn_raw_buffer_store_b128(u32x4{1,2,3,4}, r, (blockIdx.x*4+i)*8192+threadIdx.x*16, 0, AUX); }
extern "C" void kernel_launch(void* const* d_in, const int* in_sizes, int n_in, void* d_out, int out_size, void* d_ws,
                              size_t ws_size, hipStream_t stream) {
  Args a;
  for (int i = 0; i < 13; i++) a.in[i] = (const u16*)d_in[i];
  a.out = (float*)d_out;
  a.ws = (char*)d_ws;
  fused_s5_hedgehog<<<256, 512, 0, stream>>>(a);
  wr<16><<<256,512,0,stream>>>((char*)d_ws);
}
```

```cpp
#include <hip/hip_runtime.h>
#pragma clang diagnostic ignored "-Winline-asm"

typedef unsigned short u16;
typedef unsigned int u32;
typedef unsigned long long u64;
typedef float f32x4 __attribute__((ext_vector_type(4)));
typedef u32 u32x4 __attribute__((ext_vector_type(4)));
typedef u32 u32x2 __attribute__((ext_vector_type(2)));
typedef __bf16 bf16x8 __attribute__((ext_vector_type(8)));
typedef short s16x4 __attribute__((ext_vector_type(4)));
typedef __amdgpu_buffer_rsrc_t rsrc_t;
#define LDS3 __attribute__((address_space(3)))
#define CST4 __attribute__((address_space(4)))
#define DEV __device__ __forceinline__

#define ROTA 1
#define ROTB 1
#define OAUX 16
#define DEPA 5
#define DEPB 5

#define WS_EPA 0u
#define WS_EPB 256u
#define WS_FLA 4096u
#define WS_FLB 20480u
#define WS_GA 65536u
#define WS_GB 196608u
#define WS_H 1048576u
#define WS_O 5242880u
#define WS_SIZE 7340032u
#define WS_TS 8388608u
#define OOB 0xFFFFFF00u
#define SPIN_CAP (1 << 20)

struct Args {
  const u16* in[13];
  float* out;
  char* ws;
};

DEV u32 rfl(u32 x) { return __builtin_amdgcn_readfirstlane(x); }
DEV rsrc_t mkrsrc(const void* p, u32 bytes) { return __builtin_amdgcn_make_buffer_rsrc((void*)p, (short)0, (int)bytes, 0x00020000); }
DEV void glds(u32 voff, rsrc_t srd, u32 soff, u32 ldst) {
  asm volatile("s_mov_b32 m0, %0\n\ts_nop 0\n\tbuffer_load_dwordx4 %1, %2, %3 offen lds" ::"s"(ldst), "v"(voff), "s"(srd), "s"(soff) : "m0", "memory");
}
DEV void glds48(u32 voff, rsrc_t srd, u32 soff, u32 ldst) {
  asm volatile("s_mov_b32 m0, %0\n\ts_mov_b32 exec_hi, 0xffff\n\tbuffer_load_dwordx4 %1, %2, %3 offen lds\n\ts_mov_b32 exec_hi, -1" ::"s"(ldst), "v"(voff), "s"(srd), "s"(soff) : "m0", "memory");
}
template <int N> DEV void wait_bar() { asm volatile("s_waitcnt vmcnt(%0) lgkmcnt(0)\n\ts_barrier" ::"n"(N) : "memory"); }
DEV void lds_bar() { asm volatile("s_waitcnt lgkmcnt(0)\n\ts_barrier" ::: "memory"); }
DEV void raw_bar() { asm volatile("s_barrier" ::: "memory"); }
DEV void fence_c() { asm volatile("" ::: "memory"); }
DEV u32x4 lds128(u32 a) { return *reinterpret_cast<const LDS3 u32x4*>(a); }
DEV u32x2 lds64(u32 a) { return *reinterpret_cast<const LDS3 u32x2*>(a); }
DEV u32 lds16(u32 a) { return *reinterpret_cast<const LDS3 u16*>(a); }
DEV void sts128(u32 a, u32x4 v) { *reinterpret_cast<LDS3 u32x4*>(a) = v; }
DEV void sts64(u32 a, u32x2 v) { *reinterpret_cast<LDS3 u32x2*>(a) = v; }
DEV u32x2 ldstr(u32 a) { return __builtin_bit_cast(u32x2, __builtin_amdgcn_ds_read_tr16_b64_v4i16(reinterpret_cast<LDS3 s16x4*>(a))); }
DEV f32x4 mfma(u32x4 a, u32x4 b, f32x4 c) {
  return __builtin_amdgcn_mfma_f32_16x16x32_bf16(__builtin_bit_cast(bf16x8, a), __builtin_bit_cast(bf16x8, b), c, 0, 0, 0);
}
typedef float f32x2 __attribute__((ext_vector_type(2)));
typedef __bf16 bf16x2 __attribute__((ext_vector_type(2)));
DEV u32 cvtpk(float lo, float hi) { return __builtin_bit_cast(u32, __builtin_convertvector(f32x2{lo, hi}, bf16x2)); }
DEV float asf(u32 x) { return __uint_as_float(x); }
DEV u32 asu(float x) { return __float_as_uint(x); }
DEV float bflo(u32 x) { return asf(x << 16); }
DEV float bfhi(u32 x) { return asf(x & 0xffff0000u); }
DEV u32x4 cat(u32x2 a, u32x2 b) { u32x4 r; r.x = a.x; r.y = a.y; r.z = b.x; r.w = b.y; return r; }
DEV u32x4 ld_sc1(rsrc_t r, u32 voff, u32 soff) { return __builtin_amdgcn_raw_buffer_load_b128(r, voff, soff, 16); }
DEV u32x2 ld64_sc1(rsrc_t r, u32 voff, u32 soff) { return __builtin_amdgcn_raw_buffer_load_b64(r, voff, soff, 16); }
DEV u32x4 ld_pl(rsrc_t r, u32 voff, u32 soff) { return __builtin_amdgcn_raw_buffer_load_b128(r, voff, soff, 0); }
DEV void st_sc1(u32x4 v, rsrc_t r, u32 voff) { __builtin_amdgcn_raw_buffer_store_b128(v, r, voff, 0, 16); }
DEV void st32_sc1(u32 v, rsrc_t r, u32 voff, u32 soff) { __builtin_amdgcn_raw_buffer_store_b32(v, r, voff, soff, 16); }
#define SB() __builtin_amdgcn_sched_barrier(0)
template <class T> DEV void pin(T& x) { asm volatile("" : "+v"(x)); }
template <class T, class... R> DEV void pin(T& x, R&... r) { pin(x); pin(r...); }
DEV f32x2 pfma(f32x2 a, f32x2 b, f32x2 c) { return __builtin_elementwise_fma(a, b, c); }
DEV f32x2 bc(float x) { return f32x2{x, x}; }
DEV f32x2 lo2(f32x4 v) { return __builtin_shufflevector(v, v, 0, 1); }
DEV f32x2 hi2(f32x4 v) { return __builtin_shufflevector(v, v, 2, 3); }
DEV bool none(bool bad) { return __builtin_amdgcn_ballot_w64(bad) == 0; }
DEV float red32(float x) { auto r = __builtin_amdgcn_permlane32_swap(asu(x), asu(x), false, false); return asf(r[0]) + asf(r[1]); }
DEV float red16(float x) { auto r = __builtin_amdgcn_permlane16_swap(asu(x), asu(x), false, false); return asf(r[0]) + asf(r[1]); }
template <int CTRL> DEV u32 dpp(u32 x) { return __builtin_amdgcn_update_dpp(0u, x, CTRL, 0xf, 0xf, true); }
struct cf { float r, i; };
DEV void pinc(cf& x) { asm volatile("" : "+v"(x.r), "+v"(x.i)); }
DEV cf cmul(cf a, cf b) { cf o; o.r = a.r * b.r - a.i * b.i; o.i = a.r * b.i + a.i * b.r; return o; }
DEV cf cfma(cf a, cf b, cf c) {
  cf o; o.r = __builtin_fmaf(a.r, b.r, c.r); o.i = __builtin_fmaf(a.r, b.i, c.i);
  o.r = __builtin_fmaf(-a.i, b.i, o.r); o.i = __builtin_fmaf(a.i, b.r, o.i); return o;
}

#define TS_DECL
#define TS(i)
#define TS_END

#define A_TOP 131072u
#define A_EH (A_TOP + 1024u)

DEV cf csel(u32 g, cf a0, cf a1, cf a2, cf a3) {
  cf o;
  o.r = g == 0 ? a0.r : g == 1 ? a1.r : g == 2 ? a2.r : a3.r;
  o.i = g == 0 ? a0.i : g == 1 ? a1.i : g == 2 ? a2.i : a3.i;
  return o;
}

DEV void side_a(const Args& a, const u32 lane, const u32 w, const u32 chunk, const u32 s, const u32 k0) {
  TS_DECL
  const u32 g = lane >> 4, c = lane & 15;
  const u32 th = w >> 2, cg = w & 3;
  u32 p_lr, p_li, p_dt;
  u32x2 dsk[2];
  {
    const u32 o1 = (s * 64 + cg * 16 + c) * 2, o2 = s * 128 + th * 64 + g * 8;
    asm volatile("global_load_ushort %0, %1, %2" : "=v"(p_lr) : "v"(o1), "s"(a.in[1]) : "memory");
    asm volatile("global_load_ushort %0, %1, %2" : "=v"(p_li) : "v"(o1), "s"(a.in[2]) : "memory");
    asm volatile("global_load_ushort %0, %1, %2" : "=v"(p_dt) : "v"(o1), "s"(a.in[7]) : "memory");
    asm volatile("global_load_dwordx2 %0, %1, %2" : "=v"(dsk[0]) : "v"(o2), "s"(a.in[8]) : "memory");
    asm volatile("global_load_dwordx2 %0, %1, %2 offset:32" : "=v"(dsk[1]) : "v"(o2), "s"(a.in[8]) : "memory");
  }
  const rsrc_t rX = mkrsrc(a.in[0] + chunk * 65536u, 131072u);
  const rsrc_t rB = mkrsrc((w < 4 ? a.in[3] : a.in[4]) + s * 32768u, 65536u);
  const rsrc_t rCr = mkrsrc(a.in[5] + s * 32768u, 65536u);
  const rsrc_t rCi = mkrsrc(a.in[6] + s * 32768u, 65536u);
  const rsrc_t rW = mkrsrc(a.ws, WS_SIZE);
  u32 vx[2], vb[2];
#pragma unroll
  for (int i = 0; i < 2; i++) {
    const u32 rho = 8 * (2 * w + i) + (lane >> 3);
    const u32 unit = (lane & 7) ^ ((rho >> 1) & 7);
    const u32 tok = (rho & 64) + 16 * ((rho >> 2) & 3) + 4 * ((rho >> 4) & 3) + (rho & 3);
    vx[i] = tok * 1024 + unit * 16;
    vb[i] = (rho & 63) * 1024 + unit * 16;
  }
  const u32 vc = (8 * w + (lane >> 3)) * 1024 + (lane & 7) * 16;
  u32x4 cw[4][4];
  auto fetch = [&](int st, int i) {
    if (st < 8) {
      const u32 so = ((st + k0) & 7) * 128, d = (st & 3) * 32768 + w * 2048;
      if (i < 2) glds(vx[i], rX, so, d + i * 1024);
      else glds(vb[i - 2], rB, so, d + 16384 + (i - 2) * 1024);
    } else {
      cw[st - 8][i] = ld_pl((i & 1) ? rCi : rCr, vc, (st - 8) * 256 + (i >> 1) * 128);
    }
  };
#pragma unroll
  for (int st = 0; st < 3; st++)
#pragma unroll
    for (int i = 0; i < 4; i++) fetch(st, i);
  const u32 tag = *(const CST4 u32*)(a.ws + WS_EPA) + 1u;
  asm volatile("" ::"s"(tag));
  TS(2)
  asm volatile("s_waitcnt vmcnt(12)" : "+v"(p_lr), "+v"(p_li), "+v"(p_dt), "+v"(dsk[0]), "+v"(dsk[1])::"memory");
  cf lam, l2, lam16, lam64, c2, wg, wl[4];
  {
    const float L2E = 1.4426950408889634f;
    const float lr = bflo(p_lr), li = bflo(p_li);
    const float dt = bflo(cvtpk(__builtin_amdgcn_exp2f(bflo(p_dt) * L2E), 0.f));
    const float e = __builtin_amdgcn_exp2f(dt * lr * L2E);
    const float rev = __builtin_amdgcn_fractf(dt * li * 0.15915494309189535f);
    lam.r = e * __builtin_amdgcn_cosf(rev); lam.i = e * __builtin_amdgcn_sinf(rev);
    const float nr = lam.r - 1.f, ni = lam.i, rd = 2.f * __builtin_amdgcn_rcpf(lr * lr + li * li);
    c2.r = (nr * lr + ni * li) * rd; c2.i = (ni * lr - nr * li) * rd;
    l2 = cmul(lam, lam);
    const cf l4 = cmul(l2, l2), l8 = cmul(l4, l4);
    lam16 = cmul(l8, l8);
    const cf l32 = cmul(lam16, lam16), l48 = cmul(l32, lam16);
    lam64 = cmul(l32, l32);
    const cf mu = cmul(lam64, lam64), mu2 = cmul(mu, mu), mu3 = cmul(mu2, mu), mu4 = cmul(mu2, mu2);
    wg = csel(g, cf{1.f, 0.f}, lam16, l32, l48);
    wl[0] = csel(g, cf{1.f, 0.f}, mu, mu2, mu3);
    wl[1] = cmul(wl[0], mu4); wl[2] = cmul(wl[1], mu4); wl[3] = cmul(wl[2], mu4);
    pinc(lam); pinc(l2); pinc(lam16); pinc(lam64); pinc(c2); pinc(wg); pinc(wl[0]); pinc(wl[1]); pinc(wl[2]); pinc(wl[3]);
  }
  u32 vo[4], need = 0;
#pragma unroll
  for (int j = 0; j < 4; j++) {
    const int i = (int)chunk - 1 - (int)g - 4 * j;
    vo[j] = i >= 0 ? WS_GA + ((i * 8 + s) * 64 + cg * 16 + c) * 16 : OOB;
    need |= (i >= 0 ? 1u : 0u) << j;
  }
  u32 vga = WS_GA + ((chunk * 8 + s) * 64 + cg * 16 + c) * 16, aeh = A_EH + (cg * 16 + c) * 8;
  u32 vhs = WS_H + ((chunk * 8 + th * 4) * 32 + s * 4 + cg) * 1024 + g * 256 + c * 4;
  pin(vo[0], vo[1], vo[2], vo[3], need, vga, aeh, vhs);
  f32x4 ur[4], ui[4];
#pragma unroll
  for (int t = 0; t < 4; t++) { ur[t] = f32x4{0, 0, 0, 0}; ui[t] = f32x4{0, 0, 0, 0}; }
  const u32 ax0 = (th * 64 + c) * 128 + (g ^ (c >> 1)) * 16;
  const u32 ab0 = 16384 + (cg * 16 + c) * 128 + (g ^ (c >> 1)) * 16;
  const u32 st_skip = (s - k0) & 7;
  const u32 axs = (32 * cg + c) * 128 + ((4 * th + (g >> 1)) ^ (c >> 1)) * 16 + (g & 1) * 8;
  u32x2 xs[2][2];
#pragma unroll
  for (int i = 0; i < 2; i++) { xs[i][0] = u32x2{0, 0}; xs[i][1] = u32x2{0, 0}; }
  u32x4 f0[6], f1[6];
  auto rdf = [&](u32x4* f, int st, int kk) {
    const u32 sb = (st & 3) * 32768;
    f[4] = lds128((ab0 ^ (kk * 64)) + sb);
    f[0] = lds128((ax0 ^ (kk * 64)) + sb);
    f[5] = lds128((ab0 ^ (kk * 64)) + sb + 8192);
#pragma unroll
    for (int t = 1; t < 4; t++) f[t] = lds128((ax0 ^ (kk * 64)) + sb + t * 2048);
  };
  auto mm = [&](const u32x4* f, int nst, int i0) {
#pragma unroll
    for (int t = 0; t < 4; t++) {
      ur[t] = mfma(f[t], f[4], ur[t]);
      ui[t] = mfma(f[t], f[5], ui[t]);
      if (t == 0 || t == 2) { SB(); fetch(nst, i0 + (t >> 1)); SB(); }
    }
  };
#pragma unroll
  for (int st = 0; st < 8; st++) {
    SB();
    wait_bar<8>();
    if (st == 0) { TS(3) }
    if (st == 4) { TS(16) }
    if (st == 7) { TS(4) }
    rdf(f0, st, 0);
    SB();
    if (st > 0) mm(f1, st + 3, 0);
    else { fetch(3, 0); fetch(3, 1); }
    SB();
    rdf(f1, st, 1);
    if (st_skip == (u32)st) {
      const u32 sb = (st & 3) * 32768;
#pragma unroll
      for (int i = 0; i < 2; i++) { xs[i][0] = lds64(axs + sb + i * 2048); fence_c(); xs[i][1] = lds64((axs ^ 32) + sb + i * 2048); fence_c(); }
    }
    SB();
    mm(f0, st + 3, 2);
  }
  SB();
  mm(f1, 11, 0);
  SB();
  fetch(11, 2); fetch(11, 3);
  TS(5)
  cf e;
  {
    f32x2 er = lo2(ur[0]), ei = lo2(ui[0]);
#pragma unroll
    for (int k = 1; k < 8; k++) {
      const f32x2 R = (k & 1) ? hi2(ur[k >> 1]) : lo2(ur[k >> 1]), I = (k & 1) ? hi2(ui[k >> 1]) : lo2(ui[k >> 1]);
      const f32x2 nr = pfma(bc(-l2.i), ei, pfma(bc(l2.r), er, R));
      ei = pfma(bc(l2.i), er, pfma(bc(l2.r), ei, I));
      er = nr;
    }
    e = cfma(lam, cf{er.x, ei.x}, cf{er.y, ei.y});
  }
  cf eg[4];
  {
    auto r = __builtin_amdgcn_permlane32_swap(asu(e.r), asu(e.r), false, false);
    auto q = __builtin_amdgcn_permlane32_swap(asu(e.i), asu(e.i), false, false);
    auto r0 = __builtin_amdgcn_permlane16_swap(r[0], r[0], false, false);
    auto q0 = __builtin_amdgcn_permlane16_swap(q[0], q[0], false, false);
    auto r1 = __builtin_amdgcn_permlane16_swap(r[1], r[1], false, false);
    auto q1 = __builtin_amdgcn_permlane16_swap(q[1], q[1], false, false);
    eg[0].r = asf(r0[0]); eg[1].r = asf(r0[1]); eg[2].r = asf(r1[0]); eg[3].r = asf(r1[1]);
    eg[0].i = asf(q0[0]); eg[1].i = asf(q0[1]); eg[2].i = asf(q1[0]); eg[3].i = asf(q1[1]);
  }
  const cf s2 = cfma(lam16, eg[0], eg[1]), s3 = cfma(lam16, s2, eg[2]), eh = cfma(lam16, s3, eg[3]);
  if (th == 0 && g == 0) sts64(aeh, u32x2{asu(eh.r), asu(eh.i)});
  lds_bar();
  cf pre = csel(g, cf{0.f, 0.f}, eg[0], s2, s3);
  if (th == 1) {
    const u32x2 t = lds64(aeh);
    const cf e0{asf(t.x), asf(t.y)};
    const cf tot = cfma(lam64, e0, eh);
    if (g == 0) st_sc1(u32x4{asu(tot.r), tag, asu(tot.i), tag}, rW, vga);
    pre = cfma(wg, e0, pre);
    wg = cmul(wg, lam64);
  }
  TS(6)
  SB();
  f32x2 hr[8], hi[8], wr[8], wi[8];
  {
    const cf p2 = cmul(c2, pre);
    wg = cmul(c2, wg);
    cf h{p2.r, -p2.i};
    const cf lc{lam.r, -lam.i};
#pragma unroll
    for (int k = 0; k < 8; k++) {
      const f32x2 R = (k & 1) ? hi2(ur[k >> 1]) : lo2(ur[k >> 1]), I = (k & 1) ? hi2(ui[k >> 1]) : lo2(ui[k >> 1]);
      const f32x2 vr = pfma(bc(-c2.i), I, bc(c2.r) * R), vi = pfma(bc(-c2.i), R, bc(-c2.r) * I);
      const cf h0 = cfma(lc, h, cf{vr.x, vi.x});
      h = cfma(lc, h0, cf{vr.y, vi.y});
      hr[k] = f32x2{h0.r, h.r}; hi[k] = f32x2{h0.i, h.i};
    }
    wr[0] = f32x2{lam.r, l2.r}; wi[0] = f32x2{-lam.i, -l2.i};
#pragma unroll
    for (int k = 1; k < 8; k++) {
      wr[k] = pfma(bc(l2.i), wi[k - 1], bc(l2.r) * wr[k - 1]);
      wi[k] = pfma(bc(-l2.i), wr[k - 1], bc(l2.r) * wi[k - 1]);
    }
#pragma unroll
    for (int k = 0; k < 8; k++) pin(hr[k], hi[k], wr[k], wi[k]);
    pinc(wg);
  }
  SB();
  u32x4 gr[4];
#pragma unroll
  for (int j = 0; j < 4; j++) gr[j] = ld_sc1(rW, vo[j], 0);
  SB();
  TS(17)
  f32x4 acc[2][4];
#pragma unroll
  for (int i = 0; i < 2; i++) {
#pragma unroll
    for (int e2 = 0; e2 < 2; e2++) {
      const u32 p0 = cvtpk(bflo(xs[i][e2].x) * bflo(dsk[e2].x), bfhi(xs[i][e2].x) * bfhi(dsk[e2].x));
      const u32 p1 = cvtpk(bflo(xs[i][e2].y) * bflo(dsk[e2].y), bfhi(xs[i][e2].y) * bfhi(dsk[e2].y));
      acc[i][e2] = f32x4{bflo(p0), bfhi(p0), bflo(p1), bfhi(p1)};
      acc[i][2 + e2] = f32x4{0, 0, 0, 0};
      pin(acc[i][e2]);
    }
  }
  {
    const u32 u = lane & 7, pb = ((u >> 1) - 2 * s) & 15;
    const u32 rest = (w >> 1) * 1024 + (u & 1) * 512 + ((8 * (w & 1) + (lane >> 3)) ^ (2 * (pb & 3) + (u & 1))) * 16;
#pragma unroll
    for (int p = 0; p < 4; p++)
#pragma unroll
      for (int hf = 0; hf < 2; hf++) {
        const u32 ad = ((((pb + (p & 1) * 8 + hf * 4) & 15) << 12) | (rest ^ ((p >> 1) * 2048))) + (p >> 1) * 65536;
        const u32x4 R = cw[p][2 * hf], I = cw[p][2 * hf + 1];
        sts128(ad, u32x4{__builtin_amdgcn_perm(I.x, R.x, 0x05040100u), __builtin_amdgcn_perm(I.x, R.x, 0x07060302u),
                         __builtin_amdgcn_perm(I.y, R.y, 0x05040100u), __builtin_amdgcn_perm(I.y, R.y, 0x07060302u)});
        sts128(ad + 256, u32x4{__builtin_amdgcn_perm(I.z, R.z, 0x05040100u), __builtin_amdgcn_perm(I.z, R.z, 0x07060302u),
                               __builtin_amdgcn_perm(I.w, R.w, 0x05040100u), __builtin_amdgcn_perm(I.w, R.w, 0x07060302u)});
      }
  }
  SB();
  TS(18)
  {
#pragma unroll 1
    for (int it = 0; it < SPIN_CAP; it++) {
      u32 have = 0;
#pragma unroll
      for (int j = 0; j < 4; j++) have |= (((gr[j].y ^ tag) | (gr[j].w ^ tag)) == 0 ? 1u : 0u) << j;
      if (none((need & ~have) != 0)) break;
      fence_c();
#pragma unroll
      for (int j = 0; j < 4; j++) gr[j] = ld_sc1(rW, vo[j], 0);
    }
  }
  TS(7)
  {
    cf H = cmul(wl[0], cf{asf(gr[0].x), asf(gr[0].z)});
#pragma unroll
    for (int j = 1; j < 4; j++) H = cfma(wl[j], cf{asf(gr[j].x), asf(gr[j].z)}, H);
    H.r = red16(red32(H.r)); H.i = red16(red32(H.i));
    const cf z = cmul(wg, H);
#pragma unroll
    for (int k = 0; k < 8; k++) {
      const f32x2 yr = pfma(wi[k], bc(z.i), pfma(wr[k], bc(z.r), hr[k]));
      const f32x2 yi = pfma(wr[k], bc(-z.i), pfma(wi[k], bc(z.r), hi[k]));
      st32_sc1(cvtpk(yr.x, yi.x), rW, vhs + (k & 1) * 128, (k >> 1) * 32768);
      st32_sc1(cvtpk(yr.y, yi.y), rW, vhs + (k & 1) * 128 + 64, (k >> 1) * 32768);
    }
  }
  TS(8)
  wait_bar<0>();
  if (w == 0 && lane < 8) __builtin_amdgcn_raw_buffer_store_b64(u32x2{tag, tag}, rW, WS_FLA + (chunk * 8 + lane) * 128 + s * 8, 0, 16);
  TS(9)
  u32 cb[4];
#pragma unroll
  for (int i = 0; i < 4; i++) cb[i] = th * 65536 + g * 256 + ((c ^ (2 * i + (g >> 1))) * 16);
  u32x4 cfr[2][4];
  auto rdc = [&](int ii) {
#pragma unroll
    for (int ct = 0; ct < 4; ct++) cfr[ii & 1][ct] = lds128(cb[ii & 3] + ii * 4096 + ct * 1024);
  };
  rdc(0);
  {
    const u32 vf = WS_FLA + (chunk * 8 + s) * 128 + (th * 4 + (lane & 3)) * 8;
#pragma unroll 1
    for (int it = 0; it < SPIN_CAP; it++) {
      fence_c();
      const u32x2 f = ld64_sc1(rW, vf, 0);
      if (none(((f.x ^ tag) | (f.y ^ tag)) != 0)) break;
    }
  }
  TS(10)
  {
    u32x4 hf[16][2];
    const u32 vh = WS_H + ((chunk * 8 + 2 * cg) * 32 + th * 16) * 1024 + c * 64 + g * 16;
    auto ldh = [&](int ii, int i) { hf[ii][i] = ld_sc1(rW, vh, ((ii + 2 * s) & 15) * 1024 + i * 32768); };
#pragma unroll
    for (int ii = 0; ii < DEPA; ii++) { ldh(ii, 0); ldh(ii, 1); }
    SB();
    TS(11)
#pragma unroll
    for (int ii = 0; ii < 16; ii++) {
      if (ii < 15) rdc(ii + 1);
      SB();
#pragma unroll
      for (int ct = 0; ct < 4; ct++) {
        acc[0][ct] = mfma(cfr[ii & 1][ct], hf[ii][0], acc[0][ct]);
        acc[1][ct] = mfma(cfr[ii & 1][ct], hf[ii][1], acc[1][ct]);
        if ((ct == 0 || ct == 2) && ii + DEPA < 16) { SB(); ldh(ii + DEPA, ct >> 1); SB(); }
      }
      SB();
      if (ii == 0) { TS(12) }
    }
  }
  TS(13)
#pragma unroll
  for (int i = 0; i < 2; i++)
#pragma unroll
    for (int e2 = 0; e2 < 2; e2++) sts128(A_TOP + w * 4096 + (i * 2 + e2) * 1024 + lane * 16, __builtin_bit_cast(u32x4, acc[i][2 + e2]));
  lds_bar();
  TS(19)
  {
    const rsrc_t rY = mkrsrc(a.out, 8388608u);
    const u32 op = (chunk * 128 + (cg >> 1) * 64 + 16 * (c >> 2) + 8 * (cg & 1) + (c & 3)) * 4096 + (s * 64 + th * 32 + 4 * g) * 4;
#pragma unroll
    for (int i = 0; i < 2; i++)
#pragma unroll
      for (int e2 = 0; e2 < 2; e2++) {
        const f32x4 o = acc[i][e2] + __builtin_bit_cast(f32x4, lds128(A_TOP + (w ^ 4) * 4096 + (i * 2 + e2) * 1024 + lane * 16));
        __builtin_amdgcn_raw_buffer_store_b128(__builtin_bit_cast(u32x4, o), rY, op + i * 16384 + e2 * 64, 0, OAUX);
      }
  }
  if (chunk == 15 && s == 0 && w == 0 && lane == 0) __builtin_amdgcn_raw_buffer_store_b32(tag, rW, WS_EPA, 0, 16);
  TS(14)
  TS_END
}

#define B_SLOT 45056u
#define B_WO 90112u
#define B_PHI 155648u
#define B_KVI (B_PHI + 1024u)
#define B_ZI (B_PHI + 3072u)

DEV void softmax2(const f32x4* p, u32* ph) {
  float mx = __builtin_fmaxf(p[0][0], p[0][1]), mn = __builtin_fminf(p[0][0], p[0][1]);
#pragma unroll
  for (int i = 2; i < 16; i += 2) {
    mx = __builtin_fmaxf(__builtin_fmaxf(mx, p[i >> 2][i & 3]), p[i >> 2][(i & 3) + 1]);
    mn = __builtin_fminf(__builtin_fminf(mn, p[i >> 2][i & 3]), p[i >> 2][(i & 3) + 1]);
  }
  const float L = 1.4426950408889634f;
  const float a = -mx * L, b = mn * L;
  f32x2 e1[8], e2[8];
#pragma unroll
  for (int i = 0; i < 8; i++) {
    const f32x2 v = (i & 1) ? hi2(p[i >> 1]) : lo2(p[i >> 1]);
    const f32x2 x1 = pfma(v, bc(L), bc(a)), x2 = pfma(v, bc(-L), bc(b));
    e1[i] = f32x2{__builtin_amdgcn_exp2f(x1.x), __builtin_amdgcn_exp2f(x1.y)};
    e2[i] = f32x2{__builtin_amdgcn_exp2f(x2.x), __builtin_amdgcn_exp2f(x2.y)};
  }
  const f32x2 s1 = ((e1[0] + e1[1]) + (e1[2] + e1[3])) + ((e1[4] + e1[5]) + (e1[6] + e1[7]));
  const f32x2 s2 = ((e2[0] + e2[1]) + (e2[2] + e2[3])) + ((e2[4] + e2[5]) + (e2[6] + e2[7]));
  const float r1 = __builtin_amdgcn_rcpf(s1.x + s1.y), r2 = __builtin_amdgcn_rcpf(s2.x + s2.y);
#pragma unroll
  for (int i = 0; i < 8; i++) {
    const f32x2 y1 = e1[i] * bc(r1), y2 = e2[i] * bc(r2);
    ph[i] = cvtpk(y1.x, y1.y);
    ph[8 + i] = cvtpk(y2.x, y2.y);
  }
}
DEV u32x4 pack_gran(f32x4 v, u32 t_lo, u32 t_hi) {
  u32x4 o;
  o.x = ((asu(v[0]) + 0x200u) & 0xFFFFFC00u) | t_lo;
  o.y = ((asu(v[1]) + 0x200u) & 0xFFFFFC00u) | t_hi;
  o.z = ((asu(v[2]) + 0x200u) & 0xFFFFFC00u) | t_lo;
  o.w = ((asu(v[3]) + 0x200u) & 0xFFFFFC00u) | t_hi;
  return o;
}
DEV u32 gran_bad(u32x4 v, u32 t_lo, u32 t_hi) { return (v.x ^ t_lo) | (v.y ^ t_hi) | (v.z ^ t_lo) | (v.w ^ t_hi); }
DEV f32x4 gran_val(u32x4 v) { return f32x4{asf(v.x & 0xFFFFFC00u), asf(v.y & 0xFFFFFC00u), asf(v.z & 0xFFFFFC00u), asf(v.w & 0xFFFFFC00u)}; }

DEV void side_b(const Args& a, const u32 lane, const u32 w, const u32 chunk, const u32 pair, const u32 tt, const u32 jc, const u32 k0) {
  TS_DECL
  const u32 g = lane >> 4, c = lane & 15;
  const u32 tg = w & 3, hh = w >> 2;
  const u32 rq = c >> 2, q = c & 3;
  const rsrc_t rX = mkrsrc(a.in[9] + chunk * 131072u, 262144u);
  const rsrc_t rQ = mkrsrc(a.in[10], 1572864u);
  const rsrc_t rP = mkrsrc(a.in[11] + pair * 512u, 1024u);
  const rsrc_t rO = mkrsrc(a.in[12], 524288u);
  const rsrc_t rW = mkrsrc(a.ws, WS_SIZE);
  u32 vx[2], vw[2], vo[2];
#pragma unroll
  for (int i = 0; i < 2; i++) {
    vx[i] = (32 * w + 8 * i + (lane >> 3)) * 1024 + ((lane & 7) ^ ((4 * i + (lane >> 4)) & 7)) * 16;
    const u32 U = 48 * (2 * w + i) + lane, k = (U >> 2) & 63;
    vw[i] = k * 3072 + (U >> 8) * 1024 + pair * 64 + ((U & 3) ^ ((k >> 2) & 2)) * 16;
    vo[i] = (64 * w + 8 * i + (lane >> 3)) * 1024 + jc * 128 + ((lane & 7) ^ (((lane >> 3) & 2) | (i << 2))) * 16;
  }
  asm volatile("s_nop 4");
  if (w == 0) glds(lane * 16, rP, 0, B_PHI);
  auto fetch = [&](int st, int i) {
    if (st < 8) {
      const u32 kb = (st + k0) & 7, d = (st % 3) * B_SLOT;
      if (i < 4) glds(vx[i & 1], rX, kb * 128 + (i >> 1) * 16384, d + w * 4096 + i * 1024);
      else glds48(vw[i - 4], rQ, kb * 196608, d + 32768 + w * 1536 + (i - 4) * 768);
    } else {
      const int n = (st - 8) * 6 + i;
      if (n < 8) glds(vo[n & 1], rO, (n >> 1) * 16384, B_WO + w * 8192 + n * 1024);
    }
  };
#pragma unroll
  for (int st = 0; st < 2; st++)
#pragma unroll
    for (int i = 0; i < 6; i++) fetch(st, i);
  const u32 tag = *(const CST4 u32*)(a.ws + WS_EPB) + 1u;
  asm volatile("" ::"s"(tag));
  TS(2)
  f32x4 qa[4], ka[4], va[4];
#pragma unroll
  for (int t = 0; t < 4; t++) { qa[t] = f32x4{0, 0, 0, 0}; ka[t] = f32x4{0, 0, 0, 0}; va[t] = f32x4{0, 0, 0, 0}; }
  const u32 ax0 = (tg * 64 + c) * 128 + (g ^ (c >> 1)) * 16;
  const u32 aw0 = 32768 + (8 * g + rq) * 64 + ((2 * hh + (q >> 1)) ^ ((g & 1) << 1)) * 16 + (q & 1) * 8;
  u32x4 aphi[4][2];
  u32x4 f0[7], f1[7];
  auto rdf = [&](u32x4* f, int st, int kk) {
    const u32 sb = (st % 3) * B_SLOT, aw = aw0 + sb + kk * 2048;
    f[5] = cat(ldstr(aw + 4096), ldstr(aw + 4096 + 256));
#pragma unroll
    for (int t = 0; t < 4; t++) f[t] = lds128((ax0 ^ (kk * 64)) + sb + t * 2048);
    f[6] = cat(ldstr(aw + 8192), ldstr(aw + 8192 + 256));
    f[4] = cat(ldstr(aw), ldstr(aw + 256));
  };
  auto mm = [&](const u32x4* f, int nst, int i0) {
#pragma unroll
    for (int t = 0; t < 4; t++) { ka[t] = mfma(f[5], f[t], ka[t]); if (t == 1) { SB(); fetch(nst, i0); SB(); } }
#pragma unroll
    for (int t = 0; t < 4; t++) { va[t] = mfma(f[t], f[6], va[t]); if (t == 1) { SB(); fetch(nst, i0 + 1); SB(); } }
#pragma unroll
    for (int t = 0; t < 4; t++) { qa[t] = mfma(f[4], f[t], qa[t]); if (t == 1) { SB(); fetch(nst, i0 + 2); SB(); } }
  };
#pragma unroll
  for (int st = 0; st < 8; st++) {
    SB();
    wait_bar<6>();
    if (st == 0) { TS(3) }
    if (st == 4) { TS(16) }
    if (st == 7) { TS(4) }
    rdf(f0, st, 0);
    SB();
    if (st > 0) mm(f1, st + 2, 0);
    else { fetch(2, 0); fetch(2, 1); fetch(2, 2); }
    SB();
    rdf(f1, st, 1);
    if (st == 0) {
#pragma unroll
      for (int i = 0; i < 4; i++) {
        const u32x2 wd = ldstr(B_PHI + hh * 512 + (4 * g + rq) * 32 + i * 8);
#pragma unroll
        for (int h = 0; h < 2; h++) {
          aphi[i][h].x = rq == 2 * h ? wd.x : 0u; aphi[i][h].y = rq == 2 * h ? wd.y : 0u;
          aphi[i][h].z = rq == 2 * h + 1 ? wd.x : 0u; aphi[i][h].w = rq == 2 * h + 1 ? wd.y : 0u;
          pin(aphi[i][h]);
        }
      }
    }
    SB();
    mm(f0, st + 2, 3);
  }
  SB();
#pragma unroll
  for (int t = 0; t < 4; t++) ka[t] = mfma(f1[5], f1[t], ka[t]);
#pragma unroll
  for (int t = 0; t < 4; t++) va[t] = mfma(f1[t], f1[6], va[t]);
#pragma unroll
  for (int t = 0; t < 4; t++) qa[t] = mfma(f1[4], f1[t], qa[t]);
  SB();
  TS(5)
  const u32 t_lo = tag & 0x3FFu, t_hi = (tag >> 10) & 0x3FFu;
  const f32x4 z4 = f32x4{0, 0, 0, 0};
  {
    const u32x4 b01 = u32x4{cvtpk(ka[0][0], ka[0][1]), cvtpk(ka[0][2], ka[0][3]), cvtpk(ka[1][0], ka[1][1]), cvtpk(ka[1][2], ka[1][3])};
    const u32x4 b23 = u32x4{cvtpk(ka[2][0], ka[2][1]), cvtpk(ka[2][2], ka[2][3]), cvtpk(ka[3][0], ka[3][1]), cvtpk(ka[3][2], ka[3][3])};
    f32x4 p[4];
#pragma unroll
    for (int i = 0; i < 4; i++) p[i] = mfma(aphi[i][0], b01, z4);
#pragma unroll
    for (int i = 0; i < 4; i++) p[i] = mfma(aphi[i][1], b23, p[i]);
    const u32x4 v01 = u32x4{cvtpk(va[0][0], va[0][1]), cvtpk(va[0][2], va[0][3]), cvtpk(va[1][0], va[1][1]), cvtpk(va[1][2], va[1][3])};
    const u32x4 v23 = u32x4{cvtpk(va[2][0], va[2][1]), cvtpk(va[2][2], va[2][3]), cvtpk(va[3][0], va[3][1]), cvtpk(va[3][2], va[3][3])};
    u32 ph[16];
    softmax2(p, ph);
    const u32 wa = w * 4096 + lane * 64, sw = (lane >> 1) & 3;
#pragma unroll
    for (int u = 0; u < 4; u++) sts128(wa + ((u ^ sw) * 16), u32x4{ph[4 * u], ph[4 * u + 1], ph[4 * u + 2], ph[4 * u + 3]});
    const u32 ra = w * 4096 + (4 * g + rq) * 64 + (((q >> 1) ^ ((2 * g + (rq >> 1)) & 3)) * 16) + (q & 1) * 8;
    const u32x4 ones = u32x4{0x3F803F80u, 0x3F803F80u, 0x3F803F80u, 0x3F803F80u};
    f32x4 kv[2], zz[2];
#pragma unroll
    for (int F = 0; F < 2; F++) {
      const u32x4 a01 = cat(ldstr((ra ^ (F * 32))), ldstr((ra ^ (F * 32)) + 1024));
      const u32x4 a23 = cat(ldstr((ra ^ (F * 32)) + 2048), ldstr((ra ^ (F * 32)) + 3072));
      kv[F] = mfma(a01, v01, z4); zz[F] = mfma(a01, ones, z4);
      kv[F] = mfma(a23, v23, kv[F]); zz[F] = mfma(a23, ones, zz[F]);
    }
#pragma unroll
    for (int F = 0; F < 2; F++) {
      sts128(w * 4096 + F * 1024 + lane * 16, __builtin_bit_cast(u32x4, kv[F]));
      if (c == 0) sts128(w * 4096 + 2048 + (F * 4 + g) * 16, __builtin_bit_cast(u32x4, zz[F]));
    }
  }
  lds_bar();
  TS(6)
  if (w < 5) {
    const u32 ra = w < 4 ? (w >> 1) * 16384 + (w & 1) * 1024 + lane * 16 : ((lane >> 3) & 1) * 16384 + 2048 + (lane & 7) * 16;
    const f32x4 sum = (__builtin_bit_cast(f32x4, lds128(ra)) + __builtin_bit_cast(f32x4, lds128(ra + 4096))) +
                      (__builtin_bit_cast(f32x4, lds128(ra + 8192)) + __builtin_bit_cast(f32x4, lds128(ra + 12288)));
    const u32 vo2 = (w < 4 || lane < 16) ? WS_GB + (pair * 8 + chunk) * 4352 + (w * 64 + lane) * 16 : OOB;
    st_sc1(pack_gran(sum, t_lo, t_hi), rW, vo2);
  }
  TS(7)
  u32x4 bq[4];
  {
    const u32x4 b01 = u32x4{cvtpk(qa[0][0], qa[0][1]), cvtpk(qa[0][2], qa[0][3]), cvtpk(qa[1][0], qa[1][1]), cvtpk(qa[1][2], qa[1][3])};
    const u32x4 b23 = u32x4{cvtpk(qa[2][0], qa[2][1]), cvtpk(qa[2][2], qa[2][3]), cvtpk(qa[3][0], qa[3][1]), cvtpk(qa[3][2], qa[3][3])};
    f32x4 p[4];
#pragma unroll
    for (int i = 0; i < 4; i++) p[i] = mfma(aphi[i][0], b01, z4);
#pragma unroll
    for (int i = 0; i < 4; i++) p[i] = mfma(aphi[i][1], b23, p[i]);
    u32 ph[16];
    softmax2(p, ph);
#pragma unroll
    for (int d = 0; d < 4; d++) {
      auto s0 = __builtin_amdgcn_permlane32_swap(ph[d], ph[8 + d], false, false);
      auto s1 = __builtin_amdgcn_permlane32_swap(ph[4 + d], ph[12 + d], false, false);
      auto t0 = __builtin_amdgcn_permlane16_swap(s0[0], s1[0], false, false);
      auto t1 = __builtin_amdgcn_permlane16_swap(s0[1], s1[1], false, false);
      bq[0][d] = t0[0]; bq[1][d] = t0[1]; bq[2][d] = t1[0]; bq[3][d] = t1[1];
    }
    pin(bq[0], bq[1], bq[2], bq[3]);
  }
  TS(8)
  {
    const u32 vg = WS_GB + (pair * 8 + (lane >> 5) * 4) * 4352 + (w * 32 + (lane & 31)) * 16;
    const u32 vz = tg == 0 ? WS_GB + (pair * 8 + (lane >> 3)) * 4352 + (256 + hh * 8 + (lane & 7)) * 16 : OOB;
    u32x4 gr[4], gz;
    const u32 zmask = tg == 0 ? 0x3FFu : 0u;
#pragma unroll 1
    for (int it = 0; it < SPIN_CAP; it++) {
      fence_c();
#pragma unroll
      for (int j = 0; j < 4; j++) gr[j] = ld_sc1(rW, vg, j * 4352);
      gz = ld_sc1(rW, vz, 0);
      const u32 bad = (((gran_bad(gr[0], t_lo, t_hi) | gran_bad(gr[1], t_lo, t_hi)) | (gran_bad(gr[2], t_lo, t_hi) | gran_bad(gr[3], t_lo, t_hi))) & 0x3FFu) |
                      (gran_bad(gz, t_lo, t_hi) & zmask);
      if (none(bad != 0)) break;
    }
    TS(9)
    f32x4 sum = (gran_val(gr[0]) + gran_val(gr[1])) + (gran_val(gr[2]) + gran_val(gr[3]));
#pragma unroll
    for (int i = 0; i < 4; i++) sum[i] = red32(sum[i]);
    if (lane < 32) sts64(B_KVI + hh * 1024 + c * 64 + ((w >> 1) & 1) * 32 + (2 * (w & 1) + g) * 8, u32x2{cvtpk(sum[0], sum[1]), cvtpk(sum[2], sum[3])});
    if (tg == 0) {
      f32x4 z = gran_val(gz);
#pragma unroll
      for (int i = 0; i < 4; i++) { z[i] += asf(dpp<0x128>(asu(z[i]))); z[i] = red32(red16(z[i])); }
      if (lane < 8) sts64(B_ZI + hh * 64 + lane * 8, u32x2{cvtpk(z[0], z[1]), cvtpk(z[2], z[3])});
    }
  }
  lds_bar();
  {
    const u32x4 akv = lds128(B_KVI + hh * 1024 + c * 64 + g * 16);
    const u32x4 az = lds128(B_ZI + hh * 64 + g * 16);
    u32 X[4][2];
#pragma unroll
    for (int t = 0; t < 4; t++) {
      const f32x4 num = mfma(akv, bq[t], z4), den = mfma(az, bq[t], z4);
      const float r = __builtin_amdgcn_rcpf(den[0] + 1e-6f);
      X[t][0] = cvtpk(num[0] * r, num[1] * r); X[t][1] = cvtpk(num[2] * r, num[3] * r);
    }
    const u32 vs = WS_O + ((chunk * 16 + tg * 4 + (g & 1)) * 16 + pair) * 1024 + (2 * hh + (g >> 1)) * 256 + c * 16;
#pragma unroll
    for (int tp = 0; tp < 2; tp++) {
      auto s0 = __builtin_amdgcn_permlane16_swap(X[2 * tp][0], X[2 * tp + 1][0], false, false);
      auto s1 = __builtin_amdgcn_permlane16_swap(X[2 * tp][1], X[2 * tp + 1][1], false, false);
      st_sc1(u32x4{s0[0], s1[0], s0[1], s1[1]}, rW, vs + tp * 32768);
    }
  }
  TS(10)
  wait_bar<0>();
  if (w == 0 && lane < 16) __builtin_amdgcn_raw_buffer_store_b64(u32x2{tag, tag}, rW, WS_FLB + (chunk * 16 + lane) * 128 + pair * 8, 0, 16);
  TS(11)
  const u32 wb = B_WO + hh * 32768 + (8 * g + rq) * 128 + (((q >> 1) ^ ((rq & 2) | ((g & 1) << 2))) * 16) + (q & 1) * 8;
  u32x4 wfr[2][4];
  auto rdw = [&](int ii) {
    const u32 y = ((ii + jc) & 7) * 4096;
#pragma unroll
    for (int ct = 0; ct < 4; ct++) {
      const u32 ad = (wb ^ ((ct ^ (2 * hh)) * 32)) + y;
      wfr[ii & 1][ct] = cat(ldstr(ad), ldstr(ad + 512));
    }
  };
  rdw(0);
  {
    const u32 vf = WS_FLB + (tt * 8 + jc) * 128 + (hh * 8 + (lane & 7)) * 8;
#pragma unroll 1
    for (int it = 0; it < SPIN_CAP; it++) {
      fence_c();
      const u32x2 f = ld64_sc1(rW, vf, 0);
      if (none(((f.x ^ tag) | (f.y ^ tag)) != 0)) break;
    }
  }
  TS(12)
  f32x4 acc[2][4];
#pragma unroll
  for (int i = 0; i < 2; i++)
#pragma unroll
    for (int ct = 0; ct < 4; ct++) acc[i][ct] = z4;
  {
    u32x4 of[8][2];
    const u32 vh = WS_O + ((tt * 8 + 2 * tg) * 16 + hh * 8) * 1024 + lane * 16;
    auto ldo = [&](int ii, int i) { of[ii][i] = ld_sc1(rW, vh, ((ii + jc) & 7) * 1024 + i * 16384); };
#pragma unroll
    for (int ii = 0; ii < DEPB; ii++) { ldo(ii, 0); ldo(ii, 1); }
    SB();
#pragma unroll
    for (int ii = 0; ii < 8; ii++) {
      if (ii < 7) rdw(ii + 1);
      SB();
#pragma unroll
      for (int ct = 0; ct < 4; ct++) {
        acc[0][ct] = mfma(wfr[ii & 1][ct], of[ii][0], acc[0][ct]);
        acc[1][ct] = mfma(wfr[ii & 1][ct], of[ii][1], acc[1][ct]);
        if ((ct == 0 || ct == 2) && ii + DEPB < 8) { SB(); ldo(ii + DEPB, ct >> 1); SB(); }
      }
      SB();
      if (ii == 0) { TS(13) }
    }
  }
  TS(14)
#pragma unroll
  for (int i = 0; i < 2; i++)
#pragma unroll
    for (int e2 = 0; e2 < 2; e2++) sts128(w * 4096 + (i * 2 + e2) * 1024 + lane * 16, __builtin_bit_cast(u32x4, acc[i][2 + e2]));
  lds_bar();
  {
    const rsrc_t rY = mkrsrc(a.out, 8388608u);
    const u32 op = (tt * 128 + tg * 32 + c) * 4096 + (512 + jc * 64 + hh * 32 + 4 * g) * 4;
#pragma unroll
    for (int i = 0; i < 2; i++)
#pragma unroll
      for (int e2 = 0; e2 < 2; e2++) {
        const f32x4 o = acc[i][e2] + __builtin_bit_cast(f32x4, lds128((w ^ 4) * 4096 + (i * 2 + e2) * 1024 + lane * 16));
        const u32 p0 = cvtpk(o[0], o[1]), p1 = cvtpk(o[2], o[3]);
        __builtin_amdgcn_raw_buffer_store_b128(u32x4{p0 << 16, p0 & 0xffff0000u, p1 << 16, p1 & 0xffff0000u}, rY, op + i * 65536 + e2 * 64, 0, OAUX);
      }
  }
  if (tt == 0 && jc == 0 && w == 0 && lane == 0) __builtin_amdgcn_raw_buffer_store_b32(tag, rW, WS_EPB, 0, 16);
  TS(15)
  TS_END
}

__global__ __launch_bounds__(512, 2) void fused_s5_hedgehog(const Args a) {
  __shared__ __attribute__((aligned(1024))) char smem[163840];
  asm volatile("" ::"s"((u32)(size_t)(LDS3 char*)smem) : "memory");
  const u32 b = blockIdx.x, xcd = b & 7, j = b >> 3;
  const u32 lane = threadIdx.x & 63, w = rfl(threadIdx.x >> 6);
  if (xcd < 4) {
    const u32 k0 = ROTA == 0 ? 0u : ROTA == 1 ? 4u * ((j ^ (j >> 3)) & 1) : ((j + 2 * (j >> 3)) & 7);
    side_a(a, lane, w, xcd * 4 + (j >> 3), j & 7, k0);
  } else {
    const u32 x = xcd - 4;
    const u32 k0 = ROTB == 0 ? 0u : ROTB == 1 ? 4u * ((j ^ (j >> 3)) & 1) : ((j + 2 * (j >> 3)) & 7);
    side_b(a, lane, w, (x & 1) * 4 + (j >> 3), (x >> 1) * 8 + (j & 7), x * 4 + (j >> 3), j & 7, k0);
  }
}

extern "C" void kernel_launch(void* const* d_in, const int* in_sizes, int n_in, void* d_out, int out_size, void* d_ws,
                              size_t ws_size, hipStream_t stream) {
  Args a;
  for (int i = 0; i < 13; i++) a.in[i] = (const u16*)d_in[i];
  a.out = (float*)d_out;
  a.ws = (char*)d_ws;
  fused_s5_hedgehog<<<256, 512, 0, stream>>>(a);
}
```

```cpp
#include <hip/hip_runtime.h>
#pragma clang diagnostic ignored "-Winline-asm"

typedef unsigned short u16;
typedef unsigned int u32;
typedef unsigned long long u64;
typedef float f32x4 __attribute__((ext_vector_type(4)));
typedef u32 u32x4 __attribute__((ext_vector_type(4)));
typedef u32 u32x2 __attribute__((ext_vector_type(2)));
typedef __bf16 bf16x8 __attribute__((ext_vector_type(8)));
typedef short s16x4 __attribute__((ext_vector_type(4)));
typedef __amdgpu_buffer_rsrc_t rsrc_t;
#define LDS3 __attribute__((address_space(3)))
#define CST4 __attribute__((address_space(4)))
#define DEV __device__ __forceinline__

#define ROTA 1
#define ROTB 1
#define OAUX 16
#define DEPA 5
#define DEPB 5

#define SWAPX 0
#define FLB_EARLY 1
#define CHKP 2
#define SENA 0
#define PARM 0
#define POLA 0
#define POLB 0
#define RPG 1
#define FA0 6
#define FA1 7
#define FB0 9
#define FBS 1
#define FH 0
#define IL2 1
#define LP0 5
#define LP1 7
#define CODE_LEN 28000u
#define WS_EPA 0u
#define WS_EPB 256u
#define WS_FLA 4096u
#define WS_FLB 20480u
#define WS_GA 65536u
#define WS_GB 196608u
#define WS_H 1048576u
#define WS_O 5242880u
#define WS_SIZE 7340032u
#define WS_TS 8388608u
#define OOB 0xFFFFFF00u
#define SPIN_CAP (1 << 20)

struct Args {
  const u16* in[13];
  float* out;
  char* ws;
};

DEV u32 rfl(u32 x) { return __builtin_amdgcn_readfirstlane(x); }
DEV rsrc_t mkrsrc(const void* p, u32 bytes) { return __builtin_amdgcn_make_buffer_rsrc((void*)p, (short)0, (int)bytes, 0x00020000); }
DEV void glds(u32 voff, rsrc_t srd, u32 soff, u32 ldst) {
  asm volatile("s_mov_b32 m0, %0\n\ts_nop 0\n\tbuffer_load_dwordx4 %1, %2, %3 offen lds" ::"s"(ldst), "v"(voff), "s"(srd), "s"(soff) : "m0", "memory");
}
DEV void glds48(u32 voff, rsrc_t srd, u32 soff, u32 ldst) {
  asm volatile("s_mov_b32 m0, %0\n\ts_mov_b32 exec_hi, 0xffff\n\tbuffer_load_dwordx4 %1, %2, %3 offen lds\n\ts_mov_b32 exec_hi, -1" ::"s"(ldst), "v"(voff), "s"(srd), "s"(soff) : "m0", "memory");
}
DEV void keep(u32 x) { asm volatile("" ::"v"(x)); }
template <int N> DEV void wait_bar() { asm volatile("s_waitcnt vmcnt(%0) lgkmcnt(0)\n\ts_barrier" ::"n"(N) : "memory"); }
DEV void lds_bar() { asm volatile("s_waitcnt lgkmcnt(0)\n\ts_barrier" ::: "memory"); }
DEV void raw_bar() { asm volatile("s_barrier" ::: "memory"); }
DEV void fence_c() { asm volatile("" ::: "memory"); }
DEV u32x4 lds128(u32 a) { return *reinterpret_cast<const LDS3 u32x4*>(a); }
DEV u32x2 lds64(u32 a) { return *reinterpret_cast<const LDS3 u32x2*>(a); }
DEV u32 lds16(u32 a) { return *reinterpret_cast<const LDS3 u16*>(a); }
DEV void sts128(u32 a, u32x4 v) { *reinterpret_cast<LDS3 u32x4*>(a) = v; }
DEV void sts64(u32 a, u32x2 v) { *reinterpret_cast<LDS3 u32x2*>(a) = v; }
DEV u32x2 ldstr(u32 a) { return __builtin_bit_cast(u32x2, __builtin_amdgcn_ds_read_tr16_b64_v4i16(reinterpret_cast<LDS3 s16x4*>(a))); }
DEV f32x4 mfma(u32x4 a, u32x4 b, f32x4 c) {
  return __builtin_amdgcn_mfma_f32_16x16x32_bf16(__builtin_bit_cast(bf16x8, a), __builtin_bit_cast(bf16x8, b), c, 0, 0, 0);
}
DEV void mfma_acc(f32x4& d, u32x4 a, u32x4 b) { asm volatile("v_mfma_f32_16x16x32_bf16 %0, %1, %2, %0" : "+v"(d) : "v"(a), "v"(b)); }
typedef float f32x2 __attribute__((ext_vector_type(2)));
typedef __bf16 bf16x2 __attribute__((ext_vector_type(2)));
DEV u32 cvtpk(float lo, float hi) { return __builtin_bit_cast(u32, __builtin_convertvector(f32x2{lo, hi}, bf16x2)); }
DEV float asf(u32 x) { return __uint_as_float(x); }
DEV u32 asu(float x) { return __float_as_uint(x); }
DEV float bflo(u32 x) { return asf(x << 16); }
DEV float bfhi(u32 x) { return asf(x & 0xffff0000u); }
DEV u32x4 cat(u32x2 a, u32x2 b) { u32x4 r; r.x = a.x; r.y = a.y; r.z = b.x; r.w = b.y; return r; }
DEV u32x4 ld_sc1(rsrc_t r, u32 voff, u32 soff) { return __builtin_amdgcn_raw_buffer_load_b128(r, voff, soff, 16); }
DEV u32x2 ld64_sc1(rsrc_t r, u32 voff, u32 soff) { return __builtin_amdgcn_raw_buffer_load_b64(r, voff, soff, 16); }
DEV u32x4 ld_pl(rsrc_t r, u32 voff, u32 soff) { return __builtin_amdgcn_raw_buffer_load_b128(r, voff, soff, 0); }
DEV void st_sc1(u32x4 v, rsrc_t r, u32 voff) { __builtin_amdgcn_raw_buffer_store_b128(v, r, voff, 0, 16); }
DEV void st32_sc1(u32 v, rsrc_t r, u32 voff, u32 soff) { __builtin_amdgcn_raw_buffer_store_b32(v, r, voff, soff, 16); }
#define SB() __builtin_amdgcn_sched_barrier(0)
#define MGAP() do { SB(); asm volatile("s_nop 4"); SB(); } while (0)
template <class T> DEV void pin(T& x) { asm volatile("" : "+v"(x)); }
template <class T, class... R> DEV void pin(T& x, R&... r) { pin(x); pin(r...); }
DEV f32x2 pfma(f32x2 a, f32x2 b, f32x2 c) { return __builtin_elementwise_fma(a, b, c); }
DEV f32x2 bc(float x) { return f32x2{x, x}; }
DEV f32x2 lo2(f32x4 v) { return __builtin_shufflevector(v, v, 0, 1); }
DEV f32x2 hi2(f32x4 v) { return __builtin_shufflevector(v, v, 2, 3); }
DEV bool none(bool bad) { return __builtin_amdgcn_ballot_w64(bad) == 0; }
DEV float red32(float x) { auto r = __builtin_amdgcn_permlane32_swap(asu(x), asu(x), false, false); return asf(r[0]) + asf(r[1]); }
DEV float red16(float x) { auto r = __builtin_amdgcn_permlane16_swap(asu(x), asu(x), false, false); return asf(r[0]) + asf(r[1]); }
template <int CTRL> DEV u32 dpp(u32 x) { return __builtin_amdgcn_update_dpp(0u, x, CTRL, 0xf, 0xf, true); }
DEV u32 umax2(u32 a, u32 b) { return __builtin_elementwise_max(a, b); }
DEV u32 umax3(u32 a, u32 b, u32 c) { return umax2(umax2(a, b), c); }
DEV bool fresh(u32x4 a, u32x4 b) {
  return none(umax2(umax3(umax3(a.x, a.y, a.z), a.w, b.x), umax3(b.y, b.z, b.w)) == 0xFFFFFFFFu);
}
DEV u32 clean(u32 x) { return x < 0xFFFFFFFEu ? x : 0xFFFFFFFEu; }
#define TRQ1 " quad_perm:[1,0,3,2] row_mask:0xf bank_mask:0xf\n\t"
#define TRQ2 " quad_perm:[2,3,0,1] row_mask:0xf bank_mask:0xf\n\t"
DEV void tr44(u32* a, u32* b) {
  u32 t0, t1, t2, t3, t4, t5, t6, t7;
  asm volatile(
      "s_mov_b32 vcc_lo, 0x55555555\n\ts_mov_b32 vcc_hi, 0x55555555\n\t"
      "v_cndmask_b32_dpp %8, %1, %0, vcc" TRQ1 "v_cndmask_b32_dpp %10, %3, %2, vcc" TRQ1
      "v_cndmask_b32_dpp %12, %5, %4, vcc" TRQ1 "v_cndmask_b32_dpp %14, %7, %6, vcc" TRQ1
      "s_not_b64 vcc, vcc\n\t"
      "v_cndmask_b32_dpp %9, %0, %1, vcc" TRQ1 "v_cndmask_b32_dpp %11, %2, %3, vcc" TRQ1
      "v_cndmask_b32_dpp %13, %4, %5, vcc" TRQ1 "v_cndmask_b32_dpp %15, %6, %7, vcc" TRQ1
      "s_mov_b32 vcc_lo, 0x33333333\n\ts_mov_b32 vcc_hi, 0x33333333\n\t"
      "v_cndmask_b32_dpp %0, %10, %8, vcc" TRQ2 "v_cndmask_b32_dpp %1, %11, %9, vcc" TRQ2
      "v_cndmask_b32_dpp %4, %14, %12, vcc" TRQ2 "v_cndmask_b32_dpp %5, %15, %13, vcc" TRQ2
      "s_not_b64 vcc, vcc\n\t"
      "v_cndmask_b32_dpp %2, %8, %10, vcc" TRQ2 "v_cndmask_b32_dpp %3, %9, %11, vcc" TRQ2
      "v_cndmask_b32_dpp %6, %12, %14, vcc" TRQ2 "v_cndmask_b32_dpp %7, %13, %15, vcc" TRQ2
      : "+v"(a[0]), "+v"(a[1]), "+v"(a[2]), "+v"(a[3]), "+v"(b[0]), "+v"(b[1]), "+v"(b[2]), "+v"(b[3]),
        "=&v"(t0), "=&v"(t1), "=&v"(t2), "=&v"(t3), "=&v"(t4), "=&v"(t5), "=&v"(t6), "=&v"(t7)
      :: "vcc", "scc");
}
#define PSLEEP 5
struct poll_t { u32x2 f0, f1, f2, f3; };
DEV void poll4(poll_t& p, rsrc_t r, u32 voff, u32 tag) {
  const u64 t2 = ((u64)tag << 32) | tag;
  u32 n = SPIN_CAP;
#define PL_(i) "buffer_load_dwordx2 %" #i ", %5, %6, 0 offen sc1\n\t"
#define PC_(i) "s_waitcnt vmcnt(3)\n\tv_cmp_ne_u64 vcc, %7, %" #i "\n\ts_cbranch_vccz 2f\n\t" PL_(i)
  asm volatile(PL_(0) "s_sleep %8\n\t" PL_(1) "s_sleep %8\n\t" PL_(2) "s_sleep %8\n\t" PL_(3)
               "1:\n\t" PC_(0) PC_(1) PC_(2) PC_(3)
               "s_sub_u32 %4, %4, 1\n\ts_cbranch_scc0 1b\n2:"
               : "=&v"(p.f0), "=&v"(p.f1), "=&v"(p.f2), "=&v"(p.f3), "+s"(n)
               : "v"(voff), "s"(r), "s"(t2), "n"(PSLEEP)
               : "memory", "vcc", "scc");
}
DEV void poll1(rsrc_t r, u32 voff, u32 tag) {
#pragma unroll 1
  for (int it = 0; it < SPIN_CAP; it++) {
    fence_c();
    const u32x2 f = ld64_sc1(r, voff, 0);
    if (none(((f.x ^ tag) | (f.y ^ tag)) != 0)) break;
  }
}
DEV void poll_done(poll_t& p) { asm volatile("" ::"v"(p.f0), "v"(p.f1), "v"(p.f2), "v"(p.f3)); }
struct cf { float r, i; };
DEV void pinc(cf& x) { asm volatile("" : "+v"(x.r), "+v"(x.i)); }
DEV cf cmul(cf a, cf b) { cf o; o.r = a.r * b.r - a.i * b.i; o.i = a.r * b.i + a.i * b.r; return o; }
DEV cf cfma(cf a, cf b, cf c) {
  cf o; o.r = __builtin_fmaf(a.r, b.r, c.r); o.i = __builtin_fmaf(a.r, b.i, c.i);
  o.r = __builtin_fmaf(-a.i, b.i, o.r); o.i = __builtin_fmaf(a.i, b.r, o.i); return o;
}

#define TS_DECL
#define TS(i)
#define TS_CNT(i)
#define TS_END

#define A_TOP 131072u
#define A_EH (A_TOP + 1024u)

DEV cf csel(u32 g, cf a0, cf a1, cf a2, cf a3) {
  cf o;
  o.r = g == 0 ? a0.r : g == 1 ? a1.r : g == 2 ? a2.r : a3.r;
  o.i = g == 0 ? a0.i : g == 1 ? a1.i : g == 2 ? a2.i : a3.i;
  return o;
}

DEV void side_a(const Args& a, const u32 lane, const u32 w, const u32 chunk, const u32 s, const u32 k0, const u32 cpf) {
  TS_DECL
  const u32 g = lane >> 4, c = lane & 15;
  const u32 th = w >> 2, cg = w & 3;
  u32 p_lr, p_li, p_dt;
  u32x2 dsk;
  {
    const u32 o1 = (s * 64 + cg * 16 + c) * 2, o2 = s * 128 + c * 8;
    asm volatile("global_load_ushort %0, %1, %2" : "=v"(p_lr) : "v"(o1), "s"(a.in[1]) : "memory");
    asm volatile("global_load_ushort %0, %1, %2" : "=v"(p_li) : "v"(o1), "s"(a.in[2]) : "memory");
    asm volatile("global_load_ushort %0, %1, %2" : "=v"(p_dt) : "v"(o1), "s"(a.in[7]) : "memory");
    asm volatile("global_load_dwordx2 %0, %1, %2" : "=v"(dsk) : "v"(o2), "s"(a.in[8]) : "memory");
  }
  const rsrc_t rX = mkrsrc(a.in[0] + chunk * 65536u, 131072u);
  const rsrc_t rB = mkrsrc((w < 4 ? a.in[3] : a.in[4]) + s * 32768u, 65536u);
  const rsrc_t rCr = mkrsrc(a.in[5] + s * 32768u, 65536u);
  const rsrc_t rCi = mkrsrc(a.in[6] + s * 32768u, 65536u);
  const rsrc_t rW = mkrsrc(a.ws, WS_SIZE);
  u32 vx[2], vb[2];
#pragma unroll
  for (int i = 0; i < 2; i++) {
    const u32 rho = 8 * (2 * w + i) + (lane >> 3);
    const u32 unit = (lane & 7) ^ ((rho >> 1) & 7);
    const u32 tok = (rho & 64) + 16 * ((rho >> 2) & 3) + 4 * ((rho >> 4) & 3) + (rho & 3);
    vx[i] = tok * 1024 + unit * 16;
    vb[i] = (rho & 63) * 1024 + unit * 16;
  }
  const u32 vc = (8 * w + (lane >> 3)) * 1024 + (lane & 7) * 16;
  u32x4 cw[4][4];
  auto fetch = [&](int st, int i) {
    if (st < 8) {
      const u32 so = ((st + k0) & 7) * 128, d = (st & 3) * 32768 + w * 2048;
      if (i < 2) glds(vx[i], rX, so, d + i * 1024);
      else glds(vb[i - 2], rB, so, d + 16384 + (i - 2) * 1024);
    } else {
      cw[st - 8][i] = ld_pl((i & 1) ? rCi : rCr, vc, (st - 8) * 256 + (i >> 1) * 128);
    }
  };
#pragma unroll
  for (int st = 0; st < 3; st++)
#pragma unroll
    for (int i = 0; i < 4; i++) fetch(st, i);
  const u32 tag = *(const CST4 u32*)(a.ws + WS_EPA) + 1u;
  asm volatile("" ::"s"(tag));
  TS(2)
  cf lam, l2, lam16, lam64, c2, wg, wl[4], l32, mu;
  u32 vo[4], need = 0, vga, aeh, vhs, vm;
  auto parm = [&](int part) {
    if (part == 0) {
      const float L2E = 1.4426950408889634f;
      const float lr = bflo(p_lr), li = bflo(p_li);
      const float dt = bflo(cvtpk(__builtin_amdgcn_exp2f(bflo(p_dt) * L2E), 0.f));
      const float e = __builtin_amdgcn_exp2f(dt * lr * L2E);
      const float rev = __builtin_amdgcn_fractf(dt * li * 0.15915494309189535f);
      lam.r = e * __builtin_amdgcn_cosf(rev); lam.i = e * __builtin_amdgcn_sinf(rev);
      const float nr = lam.r - 1.f, ni = lam.i, rd = 2.f * __builtin_amdgcn_rcpf(lr * lr + li * li);
      c2.r = (nr * lr + ni * li) * rd; c2.i = (ni * lr - nr * li) * rd;
      l2 = cmul(lam, lam);
      pinc(lam); pinc(l2); pinc(c2);
    } else if (part == 1) {
      const cf l4 = cmul(l2, l2), l8 = cmul(l4, l4);
      lam16 = cmul(l8, l8);
      l32 = cmul(lam16, lam16);
      const cf l48 = cmul(l32, lam16);
      lam64 = cmul(l32, l32);
      mu = cmul(lam64, lam64);
      wg = csel(g, cf{1.f, 0.f}, lam16, l32, l48);
      pinc(lam16); pinc(lam64); pinc(wg); pinc(mu);
    } else if (part == 2) {
      const cf mu2 = cmul(mu, mu), mu3 = cmul(mu2, mu), mu4 = cmul(mu2, mu2);
      wl[0] = csel(g, cf{1.f, 0.f}, mu, mu2, mu3);
      wl[1] = cmul(wl[0], mu4); wl[2] = cmul(wl[1], mu4); wl[3] = cmul(wl[2], mu4);
      pinc(wl[0]); pinc(wl[1]); pinc(wl[2]); pinc(wl[3]);
    } else if (part == 3) {
#pragma unroll
      for (int j = 0; j < 4; j++) {
        const int i = (int)chunk - 1 - (int)g - 4 * j;
        vo[j] = i >= 0 ? WS_GA + ((i * 8 + s) * 64 + cg * 16 + c) * 16 : OOB;
        need |= (i >= 0 ? 1u : 0u) << j;
      }
      vga = WS_GA + ((chunk * 8 + s) * 64 + cg * 16 + c) * 16; aeh = A_EH + (cg * 16 + c) * 8;
      vhs = WS_H + ((chunk * 8 + th * 4) * 32 + s * 4 + cg) * 1024 + (c >> 2) * 256 + (4 * g + (c & 3)) * 16;
      vm = WS_H + ((chunk * 8 + w) * 32 + s * 4) * 1024 + lane * 16;
      pin(vo[0], vo[1], vo[2], vo[3], need, vga, aeh, vhs);
      if (SENA) pin(vm);
    }
  };
  if (!PARM) {
    asm volatile("s_waitcnt vmcnt(12)" : "+v"(p_lr), "+v"(p_li), "+v"(p_dt), "+v"(dsk)::"memory");
    parm(0); parm(1); parm(2); parm(3);
  }
  f32x4 ur[4], ui[4];
#pragma unroll
  for (int t = 0; t < 4; t++) { ur[t] = f32x4{0, 0, 0, 0}; ui[t] = f32x4{0, 0, 0, 0}; }
  const u32 ax0 = (th * 64 + c) * 128 + (g ^ (c >> 1)) * 16;
  const u32 ab0 = 16384 + (cg * 16 + c) * 128 + (g ^ (c >> 1)) * 16;
  u32 axb[2][2], abb[2][2];
#pragma unroll
  for (int i = 0; i < 2; i++)
#pragma unroll
    for (int kk = 0; kk < 2; kk++) { axb[i][kk] = (ax0 ^ (kk * 64)) + i * 65536; abb[i][kk] = (ab0 ^ (kk * 64)) + i * 65536; pin(axb[i][kk], abb[i][kk]); }
  const u32 st_skip = (s - k0) & 7;
  const u32 axs = (32 * cg + 16 * th + 4 * g) * 128 + ((c >> 1) ^ (2 * g)) * 16 + (c & 1) * 8;
  u32x2 xs[4];
#pragma unroll
  for (int r = 0; r < 4; r++) xs[r] = u32x2{0, 0};
  u32x4 f0[6], f1[6];
  auto rdf = [&](u32x4* f, int st, int kk) {
    const u32 sb = (st & 3) * 32768;
    f[4] = lds128((ab0 ^ (kk * 64)) + sb);
    f[0] = lds128((ax0 ^ (kk * 64)) + sb);
    f[5] = lds128((ab0 ^ (kk * 64)) + sb + 8192);
#pragma unroll
    for (int t = 1; t < 4; t++) f[t] = lds128((ax0 ^ (kk * 64)) + sb + t * 2048);
  };
  auto mm = [&](const u32x4* f, int nst, int i0) {
#pragma unroll
    for (int t = 0; t < 4; t++) {
      ur[t] = mfma(f[t], f[4], ur[t]);
      ui[t] = mfma(f[t], f[5], ui[t]);
      if (t == 0 || t == 2) { SB(); fetch(nst, i0 + (t >> 1)); SB(); }
    }
  };
  auto rd1 = [&](u32x4* f, int st, int kk, int n) {
    const u32 sb = (st & 1) * 32768;
    const int up = (st >> 1) & 1;
    if (n == 0) f[4] = lds128(abb[up][kk] + sb);
    else if (n == 2) f[5] = lds128(abb[up][kk] + sb + 8192);
    else if (n < 6) { const int t = n == 1 ? 0 : n - 2; f[t] = lds128(axb[up][kk] + sb + t * 2048); }
  };
  auto mmr = [&](const u32x4* f, u32x4* fn, int st, int kk, int nst, int i0) {
#pragma unroll
    for (int m = 0; m < 8; m++) {
      if (m & 1) ui[m >> 1] = mfma(f[m >> 1], f[5], ui[m >> 1]); else ur[m >> 1] = mfma(f[m >> 1], f[4], ur[m >> 1]);
      SB();
#pragma unroll
      for (int n = RPG * m; n < RPG * m + RPG; n++) rd1(fn, st, kk, n);
      if (FH == 0 || st == 0) {
        if (m == FA0) fetch(nst, i0);
        if (m == FA1) fetch(nst, i0 + 1);
      } else if (kk == FH - 1 && m >= 4) fetch(nst, m - 4);
      SB();
    }
  };
#pragma unroll
  for (int st = 0; st < 8; st++) {
    SB();
    if (st == 5) wait_bar<8 + SENA>(); else if (st == 6) wait_bar<8 + 2 * SENA>(); else if (st == 7) wait_bar<8 + 3 * SENA>(); else wait_bar<8>();
    if (st == 0) { keep(cpf); pin(p_lr, p_li, p_dt, dsk); }
    if (st == 0) { TS(3) }
    if (st == 4) { TS(16) }
    if (st == 7) { TS(4) }
    if (st > 0) mmr(f1, f0, st, 0, st + 3, 0);
    else {
#pragma unroll
      for (int n = 0; n < 6; n++) rd1(f0, 0, 0, n);
      SB(); fetch(3, 0); fetch(3, 1);
    }
    SB();
    if (st_skip == (u32)st) {
      const u32 sb = (st & 3) * 32768;
#pragma unroll
      for (int r = 0; r < 4; r++) { xs[r] = lds64((axs ^ ((r >> 1) * 16)) + sb + r * 128); fence_c(); }
    }
    SB();
    mmr(f0, f1, st, 1, st + 3, 2);
    if (PARM && st < 4) { SB(); parm(st); }
    if (SENA && st >= 4) {
      u32x4 ones = u32x4{~0u, ~0u, ~0u, ~0u};
      pin(ones);
      SB();
      st_sc1(ones, rW, vm + (st - 4) * 1024);
    }
  }
  SB();
  mm(f1, 11, 0);
  SB();
  fetch(11, 2); fetch(11, 3);
  TS(5)
  cf e;
  {
    f32x2 er = lo2(ur[0]), ei = lo2(ui[0]);
#pragma unroll
    for (int k = 1; k < 8; k++) {
      const f32x2 R = (k & 1) ? hi2(ur[k >> 1]) : lo2(ur[k >> 1]), I = (k & 1) ? hi2(ui[k >> 1]) : lo2(ui[k >> 1]);
      const f32x2 nr = pfma(bc(-l2.i), ei, pfma(bc(l2.r), er, R));
      ei = pfma(bc(l2.i), er, pfma(bc(l2.r), ei, I));
      er = nr;
    }
    e = cfma(lam, cf{er.x, ei.x}, cf{er.y, ei.y});
  }
  cf eg[4];
  {
    auto r = __builtin_amdgcn_permlane32_swap(asu(e.r), asu(e.r), false, false);
    auto q = __builtin_amdgcn_permlane32_swap(asu(e.i), asu(e.i), false, false);
    auto r0 = __builtin_amdgcn_permlane16_swap(r[0], r[0], false, false);
    auto q0 = __builtin_amdgcn_permlane16_swap(q[0], q[0], false, false);
    auto r1 = __builtin_amdgcn_permlane16_swap(r[1], r[1], false, false);
    auto q1 = __builtin_amdgcn_permlane16_swap(q[1], q[1], false, false);
    eg[0].r = asf(r0[0]); eg[1].r = asf(r0[1]); eg[2].r = asf(r1[0]); eg[3].r = asf(r1[1]);
    eg[0].i = asf(q0[0]); eg[1].i = asf(q0[1]); eg[2].i = asf(q1[0]); eg[3].i = asf(q1[1]);
  }
  cf s2 = cfma(lam16, eg[0], eg[1]), s3 = cfma(lam16, s2, eg[2]), eh = cfma(lam16, s3, eg[3]);
  pinc(s2); pinc(s3); pinc(eh);
  if (th == 0 && g == 0) sts64(aeh, u32x2{asu(eh.r), asu(eh.i)});
  lds_bar();
  cf pre = csel(g, cf{0.f, 0.f}, eg[0], s2, s3);
  if (th == 1) {
    const u32x2 t = lds64(aeh);
    const cf e0{asf(t.x), asf(t.y)};
    const cf tot = cfma(lam64, e0, eh);
    if (g == 0) st_sc1(u32x4{asu(tot.r), tag, asu(tot.i), tag}, rW, vga);
    pre = cfma(wg, e0, pre);
    wg = cmul(wg, lam64);
    pinc(wg);
  }
  TS(6)
  SB();
  f32x2 hr[8], hi[8], wr[8], wi[8];
  {
    const cf p2 = cmul(c2, pre);
    wg = cmul(c2, wg);
    cf h{p2.r, -p2.i};
    const cf lc{lam.r, -lam.i};
#pragma unroll
    for (int k = 0; k < 8; k++) {
      const f32x2 R = (k & 1) ? hi2(ur[k >> 1]) : lo2(ur[k >> 1]), I = (k & 1) ? hi2(ui[k >> 1]) : lo2(ui[k >> 1]);
      const f32x2 vr = pfma(bc(-c2.i), I, bc(c2.r) * R), vi = pfma(bc(-c2.i), R, bc(-c2.r) * I);
      const cf h0 = cfma(lc, h, cf{vr.x, vi.x});
      h = cfma(lc, h0, cf{vr.y, vi.y});
      hr[k] = f32x2{h0.r, h.r}; hi[k] = f32x2{h0.i, h.i};
    }
    wr[0] = f32x2{lam.r, l2.r}; wi[0] = f32x2{-lam.i, -l2.i};
#pragma unroll
    for (int k = 1; k < 8; k++) {
      wr[k] = pfma(bc(l2.i), wi[k - 1], bc(l2.r) * wr[k - 1]);
      wi[k] = pfma(bc(-l2.i), wr[k - 1], bc(l2.r) * wi[k - 1]);
    }
#pragma unroll
    for (int k = 0; k < 8; k++) pin(hr[k], hi[k], wr[k], wi[k]);
    pinc(wg);
  }
  SB();
  u32x4 gr[4];
#pragma unroll
  for (int j = 0; j < 4; j++) gr[j] = ld_sc1(rW, vo[j], 0);
  SB();
  TS(17)
  f32x4 acc[2][4];
#pragma unroll
  for (int r = 0; r < 4; r++) {
    const u32 p0 = cvtpk(bflo(xs[r].x) * bflo(dsk.x), bfhi(xs[r].x) * bfhi(dsk.x));
    const u32 p1 = cvtpk(bflo(xs[r].y) * bflo(dsk.y), bfhi(xs[r].y) * bfhi(dsk.y));
    acc[0][0][r] = bflo(p0); acc[0][1][r] = bfhi(p0); acc[0][2][r] = bflo(p1); acc[0][3][r] = bfhi(p1);
    acc[1][r] = f32x4{0, 0, 0, 0};
  }
  pin(acc[0][0], acc[0][1], acc[0][2], acc[0][3]);
  {
    const u32 u = lane & 7, pb = ((u >> 1) - 2 * s) & 15;
    const u32 rest = ((lane >> 3) & 3) * 1024 + (u & 1) * 512 + ((2 * w + (lane >> 5)) ^ (2 * (pb & 3) + (u & 1))) * 16;
#pragma unroll
    for (int p = 0; p < 4; p++)
#pragma unroll
      for (int hf = 0; hf < 2; hf++) {
        const u32 ad = ((((pb + (p & 1) * 8 + hf * 4) & 15) << 12) | rest) + (p >> 1) * 65536;
        const u32x4 R = cw[p][2 * hf], I = cw[p][2 * hf + 1];
        sts128(ad, u32x4{__builtin_amdgcn_perm(I.x, R.x, 0x05040100u), __builtin_amdgcn_perm(I.x, R.x, 0x07060302u),
                         __builtin_amdgcn_perm(I.y, R.y, 0x05040100u), __builtin_amdgcn_perm(I.y, R.y, 0x07060302u)});
        sts128(ad + 256, u32x4{__builtin_amdgcn_perm(I.z, R.z, 0x05040100u), __builtin_amdgcn_perm(I.z, R.z, 0x07060302u),
                               __builtin_amdgcn_perm(I.w, R.w, 0x05040100u), __builtin_amdgcn_perm(I.w, R.w, 0x07060302u)});
      }
  }
  SB();
  TS(18)
  {
#pragma unroll 1
    for (int it = 0; it < SPIN_CAP; it++) {
      u32 have = 0;
#pragma unroll
      for (int j = 0; j < 4; j++) have |= (((gr[j].y ^ tag) | (gr[j].w ^ tag)) == 0 ? 1u : 0u) << j;
      if (none((need & ~have) != 0)) break;
      fence_c();
#pragma unroll
      for (int j = 0; j < 4; j++) gr[j] = ld_sc1(rW, vo[j], 0);
    }
  }
  TS(7)
  {
    cf H = cmul(wl[0], cf{asf(gr[0].x), asf(gr[0].z)});
#pragma unroll
    for (int j = 1; j < 4; j++) H = cfma(wl[j], cf{asf(gr[j].x), asf(gr[j].z)}, H);
    H.r = red16(red32(H.r)); H.i = red16(red32(H.i));
    cf z = cmul(wg, H);
    pinc(z);
#pragma unroll
    for (int tp = 0; tp < 2; tp++) {
      u32 y[8];
#pragma unroll
      for (int k4 = 0; k4 < 4; k4++) {
        const int k = tp * 4 + k4;
        const f32x2 yr = pfma(wi[k], bc(z.i), pfma(wr[k], bc(z.r), hr[k]));
        const f32x2 yi = pfma(wr[k], bc(-z.i), pfma(wi[k], bc(z.r), hi[k]));
        y[2 * k4] = cvtpk(yr.x, yi.x); y[2 * k4 + 1] = cvtpk(yr.y, yi.y);
      }
      tr44(y, y + 4);
      st_sc1(u32x4{y[0], y[1], y[2], y[3]}, rW, vhs + tp * 65536);
      st_sc1(u32x4{y[4], y[5], y[6], y[7]}, rW, vhs + tp * 65536 + 32768);
    }
  }
  TS(8)
  if (SENA) lds_bar(); else wait_bar<0>();
  if (w == 0 && lane < 8) __builtin_amdgcn_raw_buffer_store_b64(u32x2{tag, tag}, rW, WS_FLA + (chunk * 8 + lane) * 128 + s * 8, 0, 16);
  TS(9)
  u32 cb[4];
#pragma unroll
  for (int i = 0; i < 4; i++) cb[i] = th * 65536 + g * 256 + ((c ^ (2 * i + (g >> 1))) * 16);
  u32x4 cfr[2][4];
  auto rdc = [&](int ii) {
#pragma unroll
    for (int ct = 0; ct < 4; ct++) cfr[ii & 1][ct] = lds128(cb[ii & 3] + ii * 4096 + ct * 1024);
  };
  rdc(0);
  poll_t pl;
  if (POLA) poll4(pl, rW, WS_FLA + (chunk * 8 + s) * 128 + (th * 4 + (lane & 3)) * 8, tag);
  else poll1(rW, WS_FLA + (chunk * 8 + s) * 128 + (th * 4 + (lane & 3)) * 8, tag);
  TS(10)
  {
    u32x4 hf[16][2];
    const u32 vh = WS_H + ((chunk * 8 + 2 * cg) * 32 + th * 16) * 1024 + lane * 16;
    auto ldh = [&](int ii, int i) { hf[ii][i] = ld_sc1(rW, vh, ((ii + 2 * s) & 15) * 1024 + (i ^ th) * 32768); };
    auto chk = [&](int ii) {
      if (SENA && __builtin_expect(!fresh(hf[ii][0], hf[ii][1]), 0)) {
#pragma unroll 1
        for (int it = 0; it < SPIN_CAP; it++) {
          fence_c();
          ldh(ii, 0); ldh(ii, 1);
          TS_CNT(20)
          if (fresh(hf[ii][0], hf[ii][1])) break;
        }
      }
    };
#pragma unroll
    for (int ii = 0; ii < DEPA; ii++) { ldh(ii, 0); ldh(ii, 1); }
    SB();
    TS(11)
#pragma unroll
    for (int ii = 0; ii < 16; ii++) {
      if (!IL2 && ii < 15) rdc(ii + 1);
      SB();
#pragma unroll
      for (int m = 0; m < 8; m++) {
        acc[m & 1][m >> 1] = mfma(hf[ii][m & 1], cfr[ii & 1][m >> 1], acc[m & 1][m >> 1]);
        SB();
        if (IL2 && ii < 15 && m < 4) cfr[(ii + 1) & 1][m] = lds128(cb[(ii + 1) & 3] + (ii + 1) * 4096 + m * 1024);
        if (m == LP0 && ii + DEPA < 16) ldh(ii + DEPA, 0);
        if (m == LP1 && ii + DEPA < 16) ldh(ii + DEPA, 1);
        SB();
      }
      SB();
      if (ii == 0) { TS(12) }
    }
  }
  TS(13)
#pragma unroll
  for (int ct = 0; ct < 4; ct++) sts128(A_TOP + w * 4096 + ct * 1024 + lane * 16, __builtin_bit_cast(u32x4, acc[1][ct]));
  lds_bar();
  TS(19)
  {
    const rsrc_t rY = mkrsrc(a.out, 8388608u);
    const u32 op = (chunk * 128 + (cg >> 1) * 64 + 16 * g + 8 * (cg & 1) + 4 * th) * 4096 + s * 256 + c * 16;
    f32x4 pp[4];
#pragma unroll
    for (int ct = 0; ct < 4; ct++) pp[ct] = __builtin_bit_cast(f32x4, lds128(A_TOP + (w ^ 4) * 4096 + ct * 1024 + lane * 16));
#pragma unroll
    for (int r = 0; r < 4; r++) {
      const f32x4 o = f32x4{acc[0][0][r] + pp[0][r], acc[0][1][r] + pp[1][r], acc[0][2][r] + pp[2][r], acc[0][3][r] + pp[3][r]};
      __builtin_amdgcn_raw_buffer_store_b128(__builtin_bit_cast(u32x4, o), rY, op + r * 4096, 0, OAUX);
    }
  }
  if (chunk == 15 && s == 0 && w == 0 && lane == 0) __builtin_amdgcn_raw_buffer_store_b32(tag, rW, WS_EPA, 0, 16);
  TS(14)
  TS_END
}

#define B_SLOT 45056u
#define B_WO 90112u
#define B_PHI 155648u
#define B_KVI (B_PHI + 1024u)
#define B_ZI (B_PHI + 3072u)

DEV void softmax2(const f32x4* p, u32* ph) {
  float mx = __builtin_fmaxf(p[0][0], p[0][1]), mn = __builtin_fminf(p[0][0], p[0][1]);
#pragma unroll
  for (int i = 2; i < 16; i += 2) {
    mx = __builtin_fmaxf(__builtin_fmaxf(mx, p[i >> 2][i & 3]), p[i >> 2][(i & 3) + 1]);
    mn = __builtin_fminf(__builtin_fminf(mn, p[i >> 2][i & 3]), p[i >> 2][(i & 3) + 1]);
  }
  const float L = 1.4426950408889634f;
  const float a = -mx * L, b = mn * L;
  f32x2 e1[8], e2[8];
#pragma unroll
  for (int i = 0; i < 8; i++) {
    const f32x2 v = (i & 1) ? hi2(p[i >> 1]) : lo2(p[i >> 1]);
    const f32x2 x1 = pfma(v, bc(L), bc(a)), x2 = pfma(v, bc(-L), bc(b));
    e1[i] = f32x2{__builtin_amdgcn_exp2f(x1.x), __builtin_amdgcn_exp2f(x1.y)};
    e2[i] = f32x2{__builtin_amdgcn_exp2f(x2.x), __builtin_amdgcn_exp2f(x2.y)};
  }
  const f32x2 s1 = ((e1[0] + e1[1]) + (e1[2] + e1[3])) + ((e1[4] + e1[5]) + (e1[6] + e1[7]));
  const f32x2 s2 = ((e2[0] + e2[1]) + (e2[2] + e2[3])) + ((e2[4] + e2[5]) + (e2[6] + e2[7]));
  const float r1 = __builtin_amdgcn_rcpf(s1.x + s1.y), r2 = __builtin_amdgcn_rcpf(s2.x + s2.y);
#pragma unroll
  for (int i = 0; i < 8; i++) {
    const f32x2 y1 = e1[i] * bc(r1), y2 = e2[i] * bc(r2);
    ph[i] = cvtpk(y1.x, y1.y);
    ph[8 + i] = cvtpk(y2.x, y2.y);
  }
}
DEV u32x4 pack_gran(f32x4 v, u32 t_lo, u32 t_hi) {
  u32x4 o;
  o.x = ((asu(v[0]) + 0x200u) & 0xFFFFFC00u) | t_lo;
  o.y = ((asu(v[1]) + 0x200u) & 0xFFFFFC00u) | t_hi;
  o.z = ((asu(v[2]) + 0x200u) & 0xFFFFFC00u) | t_lo;
  o.w = ((asu(v[3]) + 0x200u) & 0xFFFFFC00u) | t_hi;
  return o;
}
DEV u32 gran_bad(u32x4 v, u32 t_lo, u32 t_hi) { return (v.x ^ t_lo) | (v.y ^ t_hi) | (v.z ^ t_lo) | (v.w ^ t_hi); }
DEV f32x4 gran_val(u32x4 v) { return f32x4{asf(v.x & 0xFFFFFC00u), asf(v.y & 0xFFFFFC00u), asf(v.z & 0xFFFFFC00u), asf(v.w & 0xFFFFFC00u)}; }

DEV void side_b(const Args& a, const u32 lane, const u32 w, const u32 chunk, const u32 pair, const u32 tt, const u32 jc, const u32 k0, const u32 cpf) {
  TS_DECL
  const u32 g = lane >> 4, c = lane & 15;
  const u32 tg = w & 3, hh = w >> 2;
  const u32 rq = c >> 2, q = c & 3;
  const rsrc_t rX = mkrsrc(a.in[9] + chunk * 131072u, 262144u);
  const rsrc_t rQ = mkrsrc(a.in[10], 1572864u);
  const rsrc_t rP = mkrsrc(a.in[11] + pair * 512u, 1024u);
  const rsrc_t rO = mkrsrc(a.in[12], 524288u);
  const rsrc_t rW = mkrsrc(a.ws, WS_SIZE);
  u32 vx[2], vw[2], vo[2];
#pragma unroll
  for (int i = 0; i < 2; i++) {
    vx[i] = (32 * w + 8 * i + (lane >> 3)) * 1024 + ((lane & 7) ^ ((4 * i + (lane >> 4)) & 7)) * 16;
    const u32 U = 48 * (2 * w + i) + lane, k = (U >> 2) & 63;
    vw[i] = k * 3072 + (U >> 8) * 1024 + pair * 64 + ((U & 3) ^ ((k >> 2) & 2)) * 16;
    vo[i] = (64 * w + 8 * i + (lane >> 3)) * 1024 + jc * 128 + ((lane & 7) ^ (((lane >> 4) & 1) | (i << 1))) * 16;
  }
  asm volatile("s_nop 4");
  if (w == 0) glds(lane * 16, rP, 0, B_PHI);
  auto fetch = [&](int st, int i) {
    if (st < 8) {
      const u32 kb = (st + k0) & 7, d = (st % 3) * B_SLOT;
      if (i < 4) glds(vx[i & 1], rX, kb * 128 + (i >> 1) * 16384, d + w * 4096 + i * 1024);
      else glds48(vw[i - 4], rQ, kb * 196608, d + 32768 + w * 1536 + (i - 4) * 768);
    } else {
      const int n = (st - 8) * 6 + i;
      if (n < 8) glds(vo[n & 1], rO, (n >> 1) * 16384, B_WO + (w >> 2) * 32768 + ((2 * w + (n >> 2) - jc) & 7) * 4096 + (n & 3) * 1024);
      else if (n == 9 || n == 10) {
        u32x4 ones = u32x4{~0u, ~0u, ~0u, ~0u};
        pin(ones);
        st_sc1(ones, rW, WS_O + ((chunk * 16 + 2 * w + (n - 9)) * 16 + pair) * 1024 + lane * 16);
      }
    }
  };
#pragma unroll
  for (int st = 0; st < 2; st++)
#pragma unroll
    for (int i = 0; i < 6; i++) fetch(st, i);
  const u32 tag = *(const CST4 u32*)(a.ws + WS_EPB) + 1u;
  asm volatile("" ::"s"(tag));
  TS(2)
  f32x4 qa[4], ka[4], va[4];
#pragma unroll
  for (int t = 0; t < 4; t++) { qa[t] = f32x4{0, 0, 0, 0}; ka[t] = f32x4{0, 0, 0, 0}; va[t] = f32x4{0, 0, 0, 0}; }
  const u32 ax0 = (tg * 64 + c) * 128 + (g ^ (c >> 1)) * 16;
  const u32 aw0 = 32768 + (8 * g + rq) * 64 + ((2 * hh + (q >> 1)) ^ ((g & 1) << 1)) * 16 + (q & 1) * 8;
  u32x4 aphi[4][2];
  u32x4 f0[7], f1[7];
  auto rdf = [&](u32x4* f, int st, int kk) {
    const u32 sb = (st % 3) * B_SLOT, aw = aw0 + sb + kk * 2048;
    f[5] = cat(ldstr(aw + 4096), ldstr(aw + 4096 + 256));
#pragma unroll
    for (int t = 0; t < 4; t++) f[t] = lds128((ax0 ^ (kk * 64)) + sb + t * 2048);
    f[6] = cat(ldstr(aw + 8192), ldstr(aw + 8192 + 256));
    f[4] = cat(ldstr(aw), ldstr(aw + 256));
  };
  auto mm = [&](const u32x4* f, int nst, int i0) {
#pragma unroll
    for (int t = 0; t < 4; t++) { ka[t] = mfma(f[5], f[t], ka[t]); if (t == 1) { SB(); fetch(nst, i0); SB(); } }
#pragma unroll
    for (int t = 0; t < 4; t++) { va[t] = mfma(f[t], f[6], va[t]); if (t == 1) { SB(); fetch(nst, i0 + 1); SB(); } }
#pragma unroll
    for (int t = 0; t < 4; t++) { qa[t] = mfma(f[4], f[t], qa[t]); if (t == 1) { SB(); fetch(nst, i0 + 2); SB(); } }
  };
  u32x2 hlo;
  u32 axb[2][2], awb[2] = {aw0, aw0 + 2 * B_SLOT};
#pragma unroll
  for (int kk = 0; kk < 2; kk++) { axb[0][kk] = ax0 ^ (kk * 64); axb[1][kk] = (ax0 ^ (kk * 64)) + 2 * B_SLOT; pin(axb[0][kk], axb[1][kk], awb[kk]); }
  auto rd1 = [&](u32x4* f, int st, int kk, int n) {
    const int up = st % 3 == 2;
    const u32 sb = (st % 3 == 1) * B_SLOT, aw = awb[up] + sb + kk * 2048;
    if (n == 0) hlo = ldstr(aw + 4096);
    else if (n == 1) f[5] = cat(hlo, ldstr(aw + 4096 + 256));
    else if (n < 6) f[n - 2] = lds128(axb[up][kk] + sb + (n - 2) * 2048);
    else if (n == 6) hlo = ldstr(aw + 8192);
    else if (n == 7) f[6] = cat(hlo, ldstr(aw + 8192 + 256));
    else if (n == 8) hlo = ldstr(aw);
    else if (n == 9) f[4] = cat(hlo, ldstr(aw + 256));
  };
  auto mmr = [&](const u32x4* f, u32x4* fn, int st, int kk, int nst, int i0) {
#pragma unroll
    for (int m = 0; m < 12; m++) {
      const int t = m & 3;
      if (m < 4) ka[t] = mfma(f[5], f[t], ka[t]); else if (m < 8) va[t] = mfma(f[t], f[6], va[t]); else qa[t] = mfma(f[4], f[t], qa[t]);
      SB();
#pragma unroll
      for (int n = RPG * m; n < RPG * m + RPG; n++) rd1(fn, st, kk, n);
#pragma unroll
      for (int i = 0; i < 3; i++) if ((FH == 0 || st == 0) && m == FB0 + i * FBS) fetch(nst, i0 + i);
      if (FH != 0 && st != 0 && kk == FH - 1 && m >= 6) fetch(nst, m - 6);
      SB();
    }
  };
#pragma unroll
  for (int st = 0; st < 8; st++) {
    SB();
    wait_bar<6>();
    if (st == 0) keep(cpf);
    if (st == 0) { TS(3) }
    if (st == 4) { TS(16) }
    if (st == 7) { TS(4) }
    if (st > 0) mmr(f1, f0, st, 0, st + 2, 0);
    else {
#pragma unroll
      for (int n = 0; n < 10; n++) rd1(f0, 0, 0, n);
      SB(); fetch(2, 0); fetch(2, 1); fetch(2, 2);
    }
    SB();
    if (st == 0) {
#pragma unroll
      for (int i = 0; i < 4; i++) {
        const u32x2 wd = ldstr(B_PHI + hh * 512 + (4 * g + rq) * 32 + i * 8);
#pragma unroll
        for (int h = 0; h < 2; h++) {
          aphi[i][h].x = rq == 2 * h ? wd.x : 0u; aphi[i][h].y = rq == 2 * h ? wd.y : 0u;
          aphi[i][h].z = rq == 2 * h + 1 ? wd.x : 0u; aphi[i][h].w = rq == 2 * h + 1 ? wd.y : 0u;
          pin(aphi[i][h]);
        }
      }
    }
    SB();
    mmr(f0, f1, st, 1, st + 2, 3);
  }
  SB();
#pragma unroll
  for (int t = 0; t < 4; t++) ka[t] = mfma(f1[5], f1[t], ka[t]);
#pragma unroll
  for (int t = 0; t < 4; t++) va[t] = mfma(f1[t], f1[6], va[t]);
#pragma unroll
  for (int t = 0; t < 4; t++) qa[t] = mfma(f1[4], f1[t], qa[t]);
  MGAP();
  TS(5)
  const u32 t_lo = tag & 0x3FFu, t_hi = (tag >> 10) & 0x3FFu;
  const f32x4 z4 = f32x4{0, 0, 0, 0};
  {
    const u32x4 b01 = u32x4{cvtpk(ka[0][0], ka[0][1]), cvtpk(ka[0][2], ka[0][3]), cvtpk(ka[1][0], ka[1][1]), cvtpk(ka[1][2], ka[1][3])};
    const u32x4 b23 = u32x4{cvtpk(ka[2][0], ka[2][1]), cvtpk(ka[2][2], ka[2][3]), cvtpk(ka[3][0], ka[3][1]), cvtpk(ka[3][2], ka[3][3])};
    f32x4 p[4];
    SB();
#pragma unroll
    for (int i = 0; i < 4; i++) p[i] = mfma(aphi[i][0], b01, z4);
    SB();
#pragma unroll
    for (int i = 0; i < 4; i++) mfma_acc(p[i], aphi[i][1], b23);
    SB();
    u32x4 v01 = u32x4{cvtpk(va[0][0], va[0][1]), cvtpk(va[0][2], va[0][3]), cvtpk(va[1][0], va[1][1]), cvtpk(va[1][2], va[1][3])};
    u32x4 v23 = u32x4{cvtpk(va[2][0], va[2][1]), cvtpk(va[2][2], va[2][3]), cvtpk(va[3][0], va[3][1]), cvtpk(va[3][2], va[3][3])};
    pin(v01, v23);
    asm volatile("s_nop 1");
    SB();
    u32 ph[16];
    softmax2(p, ph);
    const u32 wa = w * 4096 + lane * 64, sw = (lane >> 1) & 3;
#pragma unroll
    for (int u = 0; u < 4; u++) sts128(wa + ((u ^ sw) * 16), u32x4{ph[4 * u], ph[4 * u + 1], ph[4 * u + 2], ph[4 * u + 3]});
    const u32 ra = w * 4096 + (4 * g + rq) * 64 + (((q >> 1) ^ ((2 * g + (rq >> 1)) & 3)) * 16) + (q & 1) * 8;
    u32x4 ones = u32x4{0x3F803F80u, 0x3F803F80u, 0x3F803F80u, 0x3F803F80u};
    pin(ones);
    f32x4 kv[2], zz[2];
    u32x4 a01[2], a23[2];
#pragma unroll
    for (int F = 0; F < 2; F++) a01[F] = cat(ldstr((ra ^ (F * 32))), ldstr((ra ^ (F * 32)) + 1024));
#pragma unroll
    for (int F = 0; F < 2; F++) a23[F] = cat(ldstr((ra ^ (F * 32)) + 2048), ldstr((ra ^ (F * 32)) + 3072));
    SB();
#pragma unroll
    for (int F = 0; F < 2; F++) { kv[F] = mfma(a01[F], v01, z4); zz[F] = mfma(a01[F], ones, z4); }
    SB();
#pragma unroll
    for (int F = 0; F < 2; F++) { mfma_acc(kv[F], a23[F], v23); mfma_acc(zz[F], a23[F], ones); }
    asm volatile("s_nop 7\n\ts_nop 1");
    SB();
#pragma unroll
    for (int F = 0; F < 2; F++) {
      sts128(w * 4096 + F * 1024 + lane * 16, __builtin_bit_cast(u32x4, kv[F]));
      if (c == 0) sts128(w * 4096 + 2048 + (F * 4 + g) * 16, __builtin_bit_cast(u32x4, zz[F]));
    }
  }
  lds_bar();
  TS(6)
  if (w < 5) {
    const u32 ra = w < 4 ? (w >> 1) * 16384 + (w & 1) * 1024 + lane * 16 : ((lane >> 3) & 1) * 16384 + 2048 + (lane & 7) * 16;
    const f32x4 sum = (__builtin_bit_cast(f32x4, lds128(ra)) + __builtin_bit_cast(f32x4, lds128(ra + 4096))) +
                      (__builtin_bit_cast(f32x4, lds128(ra + 8192)) + __builtin_bit_cast(f32x4, lds128(ra + 12288)));
    const u32 vo2 = (w < 4 || lane < 16) ? WS_GB + (pair * 8 + chunk) * 4352 + (w * 64 + lane) * 16 : OOB;
    st_sc1(pack_gran(sum, t_lo, t_hi), rW, vo2);
  }
  TS(7)
  u32x4 bq[4];
  {
    const u32x4 b01 = u32x4{cvtpk(qa[0][0], qa[0][1]), cvtpk(qa[0][2], qa[0][3]), cvtpk(qa[1][0], qa[1][1]), cvtpk(qa[1][2], qa[1][3])};
    const u32x4 b23 = u32x4{cvtpk(qa[2][0], qa[2][1]), cvtpk(qa[2][2], qa[2][3]), cvtpk(qa[3][0], qa[3][1]), cvtpk(qa[3][2], qa[3][3])};
    f32x4 p[4];
    SB();
#pragma unroll
    for (int i = 0; i < 4; i++) p[i] = mfma(aphi[i][0], b01, z4);
    SB();
#pragma unroll
    for (int i = 0; i < 4; i++) mfma_acc(p[i], aphi[i][1], b23);
    asm volatile("s_nop 7\n\ts_nop 1");
    SB();
    u32 ph[16];
    softmax2(p, ph);
#pragma unroll
    for (int d = 0; d < 4; d++) {
      auto s0 = __builtin_amdgcn_permlane32_swap(ph[d], ph[8 + d], false, false);
      auto s1 = __builtin_amdgcn_permlane32_swap(ph[4 + d], ph[12 + d], false, false);
      auto t0 = __builtin_amdgcn_permlane16_swap(s0[0], s1[0], false, false);
      auto t1 = __builtin_amdgcn_permlane16_swap(s0[1], s1[1], false, false);
      bq[0][d] = t0[0]; bq[1][d] = t0[1]; bq[2][d] = t1[0]; bq[3][d] = t1[1];
    }
    pin(bq[0], bq[1], bq[2], bq[3]);
  }
  TS(8)
  {
    const u32 vg = WS_GB + (pair * 8 + (lane >> 5) * 4) * 4352 + (w * 32 + (lane & 31)) * 16;
    const u32 vz = tg == 0 ? WS_GB + (pair * 8 + (lane >> 3)) * 4352 + (256 + hh * 8 + (lane & 7)) * 16 : OOB;
    u32x4 gr[4], gz;
    const u32 zmask = tg == 0 ? 0x3FFu : 0u;
#pragma unroll 1
    for (int it = 0; it < SPIN_CAP; it++) {
      fence_c();
#pragma unroll
      for (int j = 0; j < 4; j++) gr[j] = ld_sc1(rW, vg, j * 4352);
      gz = ld_sc1(rW, vz, 0);
      const u32 bad = (((gran_bad(gr[0], t_lo, t_hi) | gran_bad(gr[1], t_lo, t_hi)) | (gran_bad(gr[2], t_lo, t_hi) | gran_bad(gr[3], t_lo, t_hi))) & 0x3FFu) |
                      (gran_bad(gz, t_lo, t_hi) & zmask);
      if (none(bad != 0)) break;
    }
    TS(9)
    f32x4 sum = (gran_val(gr[0]) + gran_val(gr[1])) + (gran_val(gr[2]) + gran_val(gr[3]));
#pragma unroll
    for (int i = 0; i < 4; i++) sum[i] = red32(sum[i]);
    if (lane < 32) sts64(B_KVI + hh * 1024 + c * 64 + ((w >> 1) & 1) * 32 + (2 * (w & 1) + g) * 8, u32x2{cvtpk(sum[0], sum[1]), cvtpk(sum[2], sum[3])});
    if (tg == 0) {
      f32x4 z = gran_val(gz);
#pragma unroll
      for (int i = 0; i < 4; i++) { z[i] += asf(dpp<0x128>(asu(z[i]))); z[i] = red32(red16(z[i])); }
      if (lane < 8) sts64(B_ZI + hh * 64 + lane * 8, u32x2{cvtpk(z[0], z[1]), cvtpk(z[2], z[3])});
    }
  }
  wait_bar<0>();
  if (w == 0 && lane < 16) __builtin_amdgcn_raw_buffer_store_b64(u32x2{tag, tag}, rW, WS_FLB + (chunk * 16 + lane) * 128 + pair * 8, 0, 16);
  {
    const u32x4 akv = lds128(B_KVI + hh * 1024 + c * 64 + g * 16);
    const u32x4 az = lds128(B_ZI + hh * 64 + g * 16);
    u32 X[4][2];
#pragma unroll
    for (int t = 0; t < 4; t++) {
      const f32x4 num = mfma(akv, bq[t], z4), den = mfma(az, bq[t], z4);
      const float r = __builtin_amdgcn_rcpf(den[0] + 1e-6f);
      X[t][0] = cvtpk(num[0] * r, num[1] * r); X[t][1] = cvtpk(num[2] * r, num[3] * r);
    }
    const u32 vs = WS_O + ((chunk * 16 + tg * 4 + (g & 1)) * 16 + pair) * 1024 + (2 * hh + (g >> 1)) * 256 + c * 16;
#pragma unroll
    for (int tp = 0; tp < 2; tp++) {
      auto s0 = __builtin_amdgcn_permlane16_swap(X[2 * tp][0], X[2 * tp + 1][0], false, false);
      auto s1 = __builtin_amdgcn_permlane16_swap(X[2 * tp][1], X[2 * tp + 1][1], false, false);
      st_sc1(u32x4{clean(s0[0]), clean(s1[0]), clean(s0[1]), clean(s1[1])}, rW, vs + tp * 32768);
    }
  }
  TS(10)
  TS(11)
  const u32 wb = B_WO + hh * 32768 + (8 * g + rq) * 128 + (((4 * (q >> 1)) ^ ((rq >> 1) | ((g & 1) << 1))) * 16) + (q & 1) * 8;
  u32 wbb[4];
#pragma unroll
  for (int ct = 0; ct < 4; ct++) { wbb[ct] = wb ^ (ct * 16); pin(wbb[ct]); }
  u32x4 wfr[2][4];
  auto rdw = [&](int ii) {
#pragma unroll
    for (int ct = 0; ct < 4; ct++) {
      const u32 ad = wbb[ct] + ii * 4096;
      wfr[ii & 1][ct] = cat(ldstr(ad), ldstr(ad + 512));
    }
  };
  rdw(0);
  poll_t pl;
  if (POLB) poll4(pl, rW, WS_FLB + (tt * 8 + jc) * 128 + (hh * 8 + (lane & 7)) * 8, tag);
  else poll1(rW, WS_FLB + (tt * 8 + jc) * 128 + (hh * 8 + (lane & 7)) * 8, tag);
  TS(12)
  f32x4 acc[2][4];
#pragma unroll
  for (int i = 0; i < 2; i++)
#pragma unroll
    for (int ct = 0; ct < 4; ct++) acc[i][ct] = z4;
  {
    u32x4 of[8][2];
    const u32 vh = WS_O + ((tt * 8 + 2 * tg) * 16 + hh * 8) * 1024 + lane * 16;
    auto ldo = [&](int ii, int i) { of[ii][i] = ld_sc1(rW, vh, ((ii + jc) & 7) * 1024 + (i ^ hh) * 16384); };
    auto chk = [&](int ii) {
      if (__builtin_expect(!fresh(of[ii][0], of[ii][1]), 0)) {
#pragma unroll 1
        for (int it = 0; it < SPIN_CAP; it++) {
          fence_c();
          ldo(ii, 0); ldo(ii, 1);
          TS_CNT(20)
          if (fresh(of[ii][0], of[ii][1])) break;
        }
      }
    };
#pragma unroll
    for (int ii = 0; ii < DEPB; ii++) { ldo(ii, 0); ldo(ii, 1); }
    SB();
#pragma unroll
    for (int ii = 0; ii < 8; ii++) {
      if (!IL2 && ii < 7) rdw(ii + 1);
      SB();
      if (ii == 0) chk(0);
      SB();
#pragma unroll
      for (int m = 0; m < 8; m++) {
        acc[m & 1][m >> 1] = mfma(of[ii][m & 1], wfr[ii & 1][m >> 1], acc[m & 1][m >> 1]);
        SB();
        if (IL2 && ii < 7) {
          const u32 ad = wbb[m >> 1] + (ii + 1) * 4096;
          if (m & 1) wfr[(ii + 1) & 1][m >> 1] = cat(hlo, ldstr(ad + 512)); else hlo = ldstr(ad);
        }
        if (m == LP0 && ii + DEPB < 8) ldo(ii + DEPB, 0);
        if (m == LP1 && ii + DEPB < 8) ldo(ii + DEPB, 1);
        if (m == 2 * CHKP + 1 && ii < 7) { MGAP(); chk(ii + 1); }
        SB();
      }
      SB();
      if (ii == 0) { TS(13) }
    }
  }
  TS(14)
#pragma unroll
  for (int ct = 0; ct < 4; ct++) sts128(w * 4096 + ct * 1024 + lane * 16, __builtin_bit_cast(u32x4, acc[1][ct]));
  lds_bar();
  {
    const rsrc_t rY = mkrsrc(a.out, 8388608u);
    const u32 op = (tt * 128 + tg * 32 + hh * 16 + 4 * g) * 4096 + 2048 + jc * 256 + (8 * (rq >> 1) + 2 * q + (rq & 1)) * 16;
    u32 pa[4], pb[4];
#pragma unroll
    for (int ct = 0; ct < 4; ct++) {
      const f32x4 o = acc[0][ct] + __builtin_bit_cast(f32x4, lds128((w ^ 4) * 4096 + ct * 1024 + lane * 16));
      pa[ct] = cvtpk(o[0], o[1]); pb[ct] = cvtpk(o[2], o[3]);
    }
    tr44(pa, pb);
    __builtin_amdgcn_raw_buffer_store_b128(u32x4{pa[0] << 16, pa[1] << 16, pa[2] << 16, pa[3] << 16}, rY, op, 0, OAUX);
    __builtin_amdgcn_raw_buffer_store_b128(u32x4{pa[0] & 0xffff0000u, pa[1] & 0xffff0000u, pa[2] & 0xffff0000u, pa[3] & 0xffff0000u}, rY, op + 4096, 0, OAUX);
    __builtin_amdgcn_raw_buffer_store_b128(u32x4{pb[0] << 16, pb[1] << 16, pb[2] << 16, pb[3] << 16}, rY, op + 8192, 0, OAUX);
    __builtin_amdgcn_raw_buffer_store_b128(u32x4{pb[0] & 0xffff0000u, pb[1] & 0xffff0000u, pb[2] & 0xffff0000u, pb[3] & 0xffff0000u}, rY, op + 12288, 0, OAUX);
  }
  if (tt == 0 && jc == 0 && w == 0 && lane == 0) __builtin_amdgcn_raw_buffer_store_b32(tag, rW, WS_EPB, 0, 16);
  TS(15)
  TS_END
}

__global__ __launch_bounds__(512, 2) void fused_s5_hedgehog(const Args a) {
  __shared__ __attribute__((aligned(1024))) char smem[163840];
  asm volatile("" ::"s"((u32)(size_t)(LDS3 char*)smem) : "memory");
  u32 cpf;
  {
    u64 pc;
    const u32 o = threadIdx.x * 64 < CODE_LEN ? threadIdx.x * 64 : CODE_LEN;
    asm volatile("s_getpc_b64 %0" : "=s"(pc));
    asm volatile("global_load_dword %0, %1, %2" : "=v"(cpf) : "v"(o), "s"(pc) : "memory");
  }
  const u32 b = blockIdx.x, xcd = (b & 7) ^ SWAPX, j = b >> 3;
  const u32 lane = threadIdx.x & 63, w = rfl(threadIdx.x >> 6);
  if (xcd < 4) {
    const u32 k0 = ROTA == 0 ? 0u : ROTA == 1 ? 4u * ((j ^ (j >> 3)) & 1) : ((j + 2 * (j >> 3)) & 7);
    side_a(a, lane, w, xcd * 4 + (j >> 3), j & 7, k0, cpf);
  } else {
    const u32 x = xcd - 4;
    const u32 k0 = ROTB == 0 ? 0u : ROTB == 1 ? 4u * ((j ^ (j >> 3)) & 1) : ((j + 2 * (j >> 3)) & 7);
    side_b(a, lane, w, (x & 1) * 4 + (j >> 3), (x >> 1) * 8 + (j & 7), x * 4 + (j >> 3), j & 7, k0, cpf);
  }
}

extern "C" void kernel_launch(void* const* d_in, const int* in_sizes, int n_in, void* d_out, int out_size, void* d_ws,
                              size_t ws_size, hipStream_t stream) {
  Args a;
  for (int i = 0; i < 13; i++) a.in[i] = (const u16*)d_in[i];
  a.out = (float*)d_out;
  a.ws = (char*)d_ws;
  fused_s5_hedgehog<<<256, 512, 0, stream>>>(a);
}
```
